```python
import torch

def _softmax(x, axis=-1):
    return torch.softmax(x, dim=axis)
def _gelu(x):
    return torch.nn.functional.gelu(x, approximate="tanh")
def _silu(x):
    return torch.nn.functional.silu(x)
def _sigmoid(x):
    return torch.sigmoid(x)
def _layernorm(x, axis=-1, eps=1e-5):
    m = x.mean(dim=axis, keepdim=True)
    v = x.var(dim=axis, keepdim=True, unbiased=False)
    return (x - m) / torch.sqrt(v + eps)
def _scatter_add(tgt, idx, src, axis=0):
    return tgt.clone().index_add_(axis, idx, src)
def _segment_sum(src, seg, n):
    out = torch.zeros((n,) + src.shape[1:], dtype=src.dtype)
    return out.index_add_(0, seg, src)
def _pad(x, pw):
    flat = tuple(v for pair in reversed(pw) for v in pair)
    return torch.nn.functional.pad(x, flat)

kernel_name = 'sequential_preLN__ssm_conv_s4d__glu_gated_swiglu__prompt'

def setup_inputs():

    _w = (lambda a: (lambda t: t.to(torch.bfloat16) if t.is_floating_point() else t)(torch.as_tensor(a)))
    def _A_setup():
        Bsz, L, D, N = 1, 2048, 640, 192
        return {
            "u": _w((lambda *s: torch.randn(*s, dtype=torch.bfloat16) * 0.05)(Bsz, L, D)),
            "log_A_real": _w((lambda *s: torch.randn(*s, dtype=torch.bfloat16) * 6.4)(D, N) * 0.5),
            "A_imag": _w((lambda *s: torch.randn(*s, dtype=torch.bfloat16) * 0.05)(D, N) * 3.14),
            "C_real": _w((lambda *s: torch.randn(*s, dtype=torch.bfloat16) * 0.05)(D, N) / N ** 0.5),
            "C_imag": _w((lambda *s: torch.randn(*s, dtype=torch.bfloat16) * 0.05)(D, N) / N ** 0.5),
            "log_dt": _w((lambda *s: torch.randn(*s, dtype=torch.bfloat16) * 0.05)(D) * 0.5 - 4.0),
            "D_skip": _w((lambda *s: torch.randn(*s, dtype=torch.bfloat16) * 0.05)(D)),
        }
    _A_in = _A_setup()

    def _B_setup():
        B, S, D, F = 1, 2048, 640, 1536
        return {
            "x": _w((lambda *s: torch.randn(*s, dtype=torch.bfloat16) * 0.05)(B, S, D)),
            "w1": _w((lambda *s: torch.randn(*s, dtype=torch.bfloat16) * 0.4)(D, F) * (2.0 / D) ** 0.5),
            "w3": _w((lambda *s: torch.randn(*s, dtype=torch.bfloat16) * 0.4)(D, F) * (2.0 / D) ** 0.5),
            "w2": _w((lambda *s: torch.randn(*s, dtype=torch.bfloat16) * 0.4)(F, D) * (2.0 / F) ** 0.5),
        }
    _B_in = _B_setup()

    return {
        "A_u": _A_in["u"],
        "A_log_A_real": _A_in["log_A_real"],
        "A_A_imag": _A_in["A_imag"],
        "A_C_real": _A_in["C_real"],
        "A_C_imag": _A_in["C_imag"],
        "A_log_dt": _A_in["log_dt"],
        "A_D_skip": _A_in["D_skip"],
        "B_x": _B_in["x"],
        "B_w1": _B_in["w1"],
        "B_w3": _B_in["w3"],
        "B_w2": _B_in["w2"],
    }

def reference(A_u, A_log_A_real, A_A_imag, A_C_real, A_C_imag, A_log_dt, A_D_skip, B_x, B_w1, B_w3, B_w2):
    def _A_ref(u, log_A_real, A_imag, C_real, C_imag, log_dt, D_skip):
        L = u.shape[1]
        dt = torch.exp(log_dt)
        A = -torch.exp(log_A_real) + 1j * A_imag
        C = (C_real + 1j * C_imag) * dt[:, None]
        pos = (lambda x, dt: x.to(dt))(torch.arange(L), log_dt.dtype)
        P = torch.exp((A * dt[:, None])[:, :, None] * pos[None, None, :])
        K = 2.0 * torch.einsum("dn,dnl->dl", C, P).real
        L2 = 2 * L
        u_t = (lambda x, *d: x.permute(*d))(u, 0, 2, 1)
        Kf = (lambda x, *a, **k: torch.fft.rfft(x.float(), *a, **k))(K, L2)
        Uf = (lambda x, *a, **k: torch.fft.rfft(x.float(), *a, **k))(u_t, L2)
        y = (lambda x, *a, **k: torch.fft.irfft(x, *a, **k).to(torch.bfloat16))(Uf * Kf[None, :, :], L2)[:, :, :L]
        y = (lambda x, *d: x.permute(*d))(y, 0, 2, 1)
        return y + u * D_skip[None, None, :]

    def _B_ref(x, w1, w3, w2):
        gate = torch.einsum("bsd,df->bsf", x, w1)
        up = torch.einsum("bsd,df->bsf", x, w3)
        h = _silu(gate) * up
        out = torch.einsum("bsf,fd->bsd", h, w2)
        return out

    x = A_u
    h = x + _A_ref(_layernorm(x, axis=-1), A_log_A_real, A_A_imag, A_C_real, A_C_imag, A_log_dt, A_D_skip)
    h2 = h + _B_ref(_layernorm(h, axis=-1), B_w1, B_w3, B_w2)
    return h2
```

```cpp
#include <hip/hip_runtime.h>
#include <hip/hip_bf16.h>
#include <stdint.h>

typedef unsigned short u16;
constexpr int L = 2048, D = 640, NS = 192, F = 1536;

__device__ __forceinline__ float bf2f(u16 v) { return __uint_as_float(((unsigned)v) << 16); }
__device__ __forceinline__ u16 f2bf(float f) { __hip_bfloat16 b = __float2bfloat16(f); return *reinterpret_cast<u16*>(&b); }
__device__ __forceinline__ float rbf(float f) { return bf2f(f2bf(f)); }

__global__ void ln_kernel(const u16* __restrict__ x, u16* __restrict__ out) {
    int row = blockIdx.x; int lane = threadIdx.x;
    float v[10]; float s = 0.f;
    for (int i = 0; i < 10; ++i) { v[i] = bf2f(x[row * D + lane + 64 * i]); s += v[i]; }
    for (int o = 32; o > 0; o >>= 1) s += __shfl_xor(s, o);
    float mean = s / D; float q = 0.f;
    for (int i = 0; i < 10; ++i) { float d = v[i] - mean; q += d * d; }
    for (int o = 32; o > 0; o >>= 1) q += __shfl_xor(q, o);
    float mb = rbf(mean), vb = rbf(q / D);
    float sd = rbf(sqrtf(rbf(vb + 1e-5f)));
    for (int i = 0; i < 10; ++i) out[row * D + lane + 64 * i] = f2bf(rbf(v[i] - mb) / sd);
}

__global__ void kgen_kernel(const u16* lar, const u16* aim, const u16* cre, const u16* cim, const u16* ldt, float* K) {
    int d = blockIdx.x;
    float dt = rbf(expf(bf2f(ldt[d])));
    for (int l = threadIdx.x; l < L; l += blockDim.x) {
        float pos = rbf((float)l);
        float acc = 0.f;
        for (int n = 0; n < NS; ++n) {
            float ar = -rbf(expf(bf2f(lar[d * NS + n]))) * dt;
            float ai = bf2f(aim[d * NS + n]) * dt;
            float cr = bf2f(cre[d * NS + n]) * dt, ci = bf2f(cim[d * NS + n]) * dt;
            float e = expf(ar * pos); float s, c; sincosf(ai * pos, &s, &c);
            acc += cr * e * c - ci * e * s;
        }
        K[d * L + l] = 2.f * acc;
    }
}

__global__ void conv_kernel(const u16* x, const u16* u, const float* K, const u16* dskip, u16* h) {
    int d = blockIdx.x;
    __shared__ float su[L], sk[L];
    for (int l = threadIdx.x; l < L; l += blockDim.x) { su[l] = bf2f(u[l * D + d]); sk[l] = K[d * L + l]; }
    __syncthreads();
    float ds = bf2f(dskip[d]);
    for (int l = threadIdx.x; l < L; l += blockDim.x) {
        float acc = 0.f;
        for (int t = 0; t <= l; ++t) acc += sk[t] * su[l - t];
        float y = rbf(acc);
        float a = rbf(y + rbf(su[l] * ds));
        h[l * D + d] = f2bf(bf2f(x[l * D + d]) + a);
    }
}

__global__ void gemm1_kernel(const u16* a, const u16* w1, const u16* w3, u16* H) {
    int row = blockIdx.y; int f = blockIdx.x * blockDim.x + threadIdx.x;
    float g = 0.f, u = 0.f;
    for (int k = 0; k < D; ++k) { float av = bf2f(a[row * D + k]); g += av * bf2f(w1[k * F + f]); u += av * bf2f(w3[k * F + f]); }
    g = rbf(g); u = rbf(u);
    float s = rbf(g / (1.f + expf(-g)));
    H[row * F + f] = f2bf(s * u);
}

__global__ void gemm2_kernel(const u16* H, const u16* w2, const u16* h, u16* out) {
    int row = blockIdx.y; int c = blockIdx.x * blockDim.x + threadIdx.x;
    float o = 0.f;
    for (int k = 0; k < F; ++k) o += bf2f(H[row * F + k]) * bf2f(w2[k * D + c]);
    out[row * D + c] = f2bf(bf2f(h[row * D + c]) + rbf(o));
}

extern "C" void kernel_launch(void* const* d_in, const int* in_sizes, int n_in,
                              void* d_out, int out_size, void* d_ws, size_t ws_size,
                              hipStream_t stream) {
    const u16* x = (const u16*)d_in[0];
    char* ws = (char*)d_ws;
    u16* u = (u16*)ws; ws += L * D * 2;
    u16* h = (u16*)ws; ws += L * D * 2;
    u16* h_ln = (u16*)ws; ws += L * D * 2;
    u16* H = (u16*)ws; ws += L * F * 2;
    float* K = (float*)ws;
    ln_kernel<<<L, 64, 0, stream>>>(x, u);
    kgen_kernel<<<D, 256, 0, stream>>>((const u16*)d_in[1], (const u16*)d_in[2], (const u16*)d_in[3], (const u16*)d_in[4], (const u16*)d_in[5], K);
    conv_kernel<<<D, 256, 0, stream>>>(x, u, K, (const u16*)d_in[6], h);
    ln_kernel<<<L, 64, 0, stream>>>(h, h_ln);
    gemm1_kernel<<<dim3(F / 256, L), 256, 0, stream>>>(h_ln, (const u16*)d_in[8], (const u16*)d_in[9], H);
    gemm2_kernel<<<dim3(D / 128, L), 128, 0, stream>>>(H, (const u16*)d_in[10], h, (u16*)d_out);
}
```

```cpp
#include <hip/hip_runtime.h>
#include <stdint.h>

typedef unsigned short u16;
typedef unsigned int u32;
typedef float f2 __attribute__((ext_vector_type(2)));
typedef float f4 __attribute__((ext_vector_type(4)));
typedef float f16v __attribute__((ext_vector_type(16)));
typedef _Float16 h2 __attribute__((ext_vector_type(2)));
typedef _Float16 h8 __attribute__((ext_vector_type(8)));
typedef __bf16 b2 __attribute__((ext_vector_type(2)));
typedef __bf16 b8 __attribute__((ext_vector_type(8)));
typedef short s4 __attribute__((ext_vector_type(4)));
typedef u32 u2 __attribute__((ext_vector_type(2)));
typedef u32 u4 __attribute__((ext_vector_type(4)));
typedef __amdgpu_buffer_rsrc_t rsrc_t;
#define LAS __attribute__((address_space(3)))
#define DEVI __device__ __forceinline__

constexpr int L = 2048, D = 640, NS = 192, F = 1536;

constexpr int WS_EPOCH = 0;
constexpr int WS_FLAG1 = 4096;
constexpr int WS_FLAG2 = 8192;
constexpr int WS_STATS = 16384;
constexpr int WS_CS    = 32768;
constexpr int WS_H4    = 65536;
constexpr int WS_HB    = WS_H4 + L * D * 2;
constexpr int WS_END   = WS_HB + L * F * 2;
constexpr size_t WS_TS = 32u << 20;
constexpr int SC1 = 16;
constexpr u32 SPIN_CAP = 1u << 14;

constexpr int LDS_BYTES = 163840;
constexpr int G1_IMG3 = 61568;
constexpr int G1_CS   = 123136;
constexpr int CV_TAB = 16512, CV_KF = 4160, CV_U = 4608;
constexpr int CV_KF0 = LDS_BYTES - 4 * CV_KF;
constexpr int CV_U0  = CV_KF0 - 4 * CV_U;
constexpr int CV_TAB0 = CV_U0 - 4 * CV_TAB;
static_assert(CV_TAB0 >= 61440, "w1 image must stay free during the conv");
constexpr int G2_SLOT = 20480, G2_NSLOT = 7;

DEVI float bf_lo(u32 v) { return __uint_as_float(v << 16); }
DEVI float bf_hi(u32 v) { return __uint_as_float(v & 0xffff0000u); }
DEVI u32 pk_bf(float a, float b) { b2 r = {(__bf16)a, (__bf16)b}; return __builtin_bit_cast(u32, r); }
DEVI u32 pk_bf(f2 a) { return pk_bf(a.x, a.y); }
DEVI f2 un_bf(u32 v) { return f2{bf_lo(v), bf_hi(v)}; }
DEVI u32 pk_h(float a, float b) { h2 r = {(_Float16)a, (_Float16)b}; return __builtin_bit_cast(u32, r); }
DEVI float rbf(float a) { return bf_lo(pk_bf(a, a)); }
DEVI float dot2(u32 a, u32 b, float c) { return __builtin_amdgcn_fdot2_f32_bf16(__builtin_bit_cast(b2, a), __builtin_bit_cast(b2, b), c, false); }

#define STAMP(i)

DEVI u32 ln_pack(float sum, float sq) {
    const float mean = sum * (1.f / D);
    const float var = fmaxf(sq * (1.f / D) - mean * mean, 0.f);
    const float sd = __builtin_amdgcn_sqrtf(rbf(rbf(var) + 1e-5f));
    return pk_bf(mean, sd);
}
DEVI void glds16(const void* sbase, u32 voff, u32 lds_dst) {
    asm volatile("s_mov_b32 m0, %2\n\ts_nop 0\n\tglobal_load_lds_dwordx4 %0, %1" :: "v"(voff), "s"(sbase), "s"(lds_dst) : "memory");
}
DEVI const char* uni(const void* p) {
    const u32 lo = __builtin_amdgcn_readfirstlane((u32)(size_t)p), hi = __builtin_amdgcn_readfirstlane((u32)((size_t)p >> 32));
    size_t r = ((size_t)hi << 32) | lo;
    asm volatile("s_nop 4" : "+s"(r));
    return (const char*)r;
}
#define WAITV(n) asm volatile("s_waitcnt vmcnt(" #n ")" ::: "memory")
#define BAR() do { asm volatile("s_waitcnt lgkmcnt(0)" ::: "memory"); __builtin_amdgcn_s_barrier(); asm volatile("" ::: "memory"); } while (0)
DEVI s4 trd(const char* p) { return __builtin_amdgcn_ds_read_tr16_b64_v4i16((LAS s4*)p); }
DEVI b8 cat(s4 a, s4 b) { return __builtin_bit_cast(b8, __builtin_shufflevector(a, b, 0, 1, 2, 3, 4, 5, 6, 7)); }
DEVI u4 ld16(rsrc_t r, int off) { return __builtin_amdgcn_raw_buffer_load_b128(r, off, 0, SC1); }
DEVI u2 ld8(rsrc_t r, int off) { return __builtin_amdgcn_raw_buffer_load_b64(r, off, 0, SC1); }
DEVI u32 ld4(rsrc_t r, int off) { return __builtin_amdgcn_raw_buffer_load_b32(r, off, 0, SC1); }
DEVI void st16(rsrc_t r, int off, u4 v) { __builtin_amdgcn_raw_buffer_store_b128(v, r, off, 0, SC1); }
DEVI void st8(rsrc_t r, int off, u2 v) { __builtin_amdgcn_raw_buffer_store_b64(v, r, off, 0, SC1); }
DEVI void st4(rsrc_t r, int off, u32 v) { __builtin_amdgcn_raw_buffer_store_b32(v, r, off, 0, SC1); }

DEVI f2 cmul(f2 a, f2 z, f2 zs) { return a.xx * z + a.yy * zs; }
DEVI f2 cexp_rev(float lg2, float rev) {
    const float m = __builtin_amdgcn_exp2f(lg2), r = __builtin_amdgcn_fractf(rev);
    return f2{m * __builtin_amdgcn_cosf(r), m * __builtin_amdgcn_sinf(r)};
}
DEVI void stage_w13(const u16* W, int nt, int qw, u32 lds_img, int lane) {
    const char* src = uni((const char*)W + nt * 96 + (size_t)qw * 160 * (F * 2));
    u32 vo[3];
#pragma unroll
    for (int b = 0; b < 3; ++b) { const int pc = b * 64 + lane; vo[b] = (pc / 6) * (F * 2) + (pc % 6) * 16; }
#pragma unroll
    for (int a = 0; a < 5; ++a)
#pragma unroll
        for (int b = 0; b < 3; ++b) glds16(src + (size_t)a * 32 * (F * 2), vo[b], lds_img + qw * 15360 + (a * 3 + b) * 1024);
}

__global__ __launch_bounds__(512, 2) void k_all(const u16* __restrict__ x, const u16* __restrict__ lar, const u16* __restrict__ aim,
                                                const u16* __restrict__ cre, const u16* __restrict__ cim, const u16* __restrict__ ldt,
                                                const u16* __restrict__ dsk, const u16* __restrict__ w1, const u16* __restrict__ w3,
                                                const u16* __restrict__ w2, char* __restrict__ ws, u16* __restrict__ out) {
    extern __shared__ __attribute__((aligned(16))) char smem[];
    STAMP(0);
    const int tid = threadIdx.x, lane = tid & 63;
    const int w = __builtin_amdgcn_readfirstlane(tid >> 6);
    const int xcd = blockIdx.x & 7, jj = blockIdx.x >> 3;
    const u32 lds0 = (u32)(size_t)(LAS char*)smem;
    const rsrc_t wsr = __builtin_amdgcn_make_buffer_rsrc(ws, 0, WS_END, 0x00020000);
    const u32 tagv = ld4(wsr, WS_EPOCH) + 1u;

    if (jj >= 20) {
        const int nb = xcd * 12 + (jj - 20);
        const int r0 = (nb * 64) / 3, r1 = ((nb + 1) * 64) / 3;
        const int row = min(r0 + w * 4 + (lane >> 4), r1 - 1);
        u4 v[5], cv[5];
        if (w < 6) {
#pragma unroll
            for (int i = 0; i < 5; ++i) v[i] = *(const u4*)(x + (size_t)row * D + ((lane & 15) + 16 * i) * 8);
        }
        const u16* W = (nb < 48) ? w1 : w3;
#pragma unroll
        for (int i = 0; i < 5; ++i) cv[i] = *(const u4*)(W + (size_t)((tid >> 2) + 128 * i) * F + (nb % 48) * 32 + (tid & 3) * 8);
        if (w < 6) {
            float s = 0.f, q = 0.f;
#pragma unroll
            for (int i = 0; i < 5; ++i)
#pragma unroll
                for (int j = 0; j < 4; ++j) { s = dot2(v[i][j], 0x3f803f80u, s); q = dot2(v[i][j], v[i][j], q); }
#pragma unroll
            for (int o = 8; o > 0; o >>= 1) { s += __shfl_xor(s, o); q += __shfl_xor(q, o); }
            if ((lane & 15) == 0) st8(wsr, WS_STATS + row * 8, u2{ln_pack(s, q), tagv});
        }
        STAMP(1);
        f2 a[4] = {};
#pragma unroll
        for (int i = 0; i < 5; ++i)
#pragma unroll
            for (int j = 0; j < 4; ++j) a[j] += un_bf(cv[i][j]);
#pragma unroll
        for (int j = 0; j < 4; ++j)
#pragma unroll
            for (int o = 4; o < 64; o <<= 1) { a[j].x += __shfl_xor(a[j].x, o); a[j].y += __shfl_xor(a[j].y, o); }
        float* red = (float*)(smem + G1_CS + 512);
        if (lane < 4)
#pragma unroll
            for (int j = 0; j < 4; ++j) *(f2*)(red + w * 32 + lane * 8 + 2 * j) = a[j];
        __syncthreads();
        if (tid < 32) {
            float s = 0.f;
#pragma unroll
            for (int k = 0; k < 8; ++k) s += red[k * 32 + tid];
            st8(wsr, WS_CS + (nb * 32 + tid) * 8, u2{__float_as_uint(s), tagv});
        }
        stage_w13(w < 4 ? w1 : w3, jj, w & 3, lds0 + (w < 4 ? 0 : G1_IMG3), lane);
        STAMP(2);
    } else {
        const int grp = xcd * 20 + jj;
        f4 acc[8] = {};
        float yscale = 0.f;
        u2 xr[4][2];
        u32 ub[4][4];
        u2 dskv;
        const int t = tid & 255;
        if (w < 4) {
            const int d = grp * 4 + w;
            char* tab = smem + CV_TAB0 + w * CV_TAB;
            char* kf = smem + CV_KF0 + w * CV_KF;
            u16 p_lar[3], p_aim[3], p_cre[3], p_cim[3];
#pragma unroll
            for (int i = 0; i < 3; ++i) {
                const int o = d * NS + lane + 64 * i;
                p_lar[i] = lar[o]; p_aim[i] = aim[o]; p_cre[i] = cre[o]; p_cim[i] = cim[o];
            }
            const float dt = rbf(expf(bf_lo(ldt[d])));
            float cmax = 0.f;
#pragma unroll
            for (int i = 0; i < 3; ++i) cmax = fmaxf(cmax, fmaxf(fabsf(bf_lo(p_cre[i])), fabsf(bf_lo(p_cim[i]))));
#pragma unroll
            for (int o = 32; o > 0; o >>= 1) cmax = fmaxf(cmax, __shfl_xor(cmax, o));
            const int ce = __builtin_amdgcn_frexp_expf(cmax);
            yscale = __builtin_amdgcn_ldexpf(2.f * dt, ce - 7);
            STAMP(1);
            f16v kacc = {};
#pragma unroll 1
            for (int i = 0; i < 3; ++i) {
                const u16 vl = i == 0 ? p_lar[0] : i == 1 ? p_lar[1] : p_lar[2];
                const u16 va = i == 0 ? p_aim[0] : i == 1 ? p_aim[1] : p_aim[2];
                const u16 vr = i == 0 ? p_cre[0] : i == 1 ? p_cre[1] : p_cre[2];
                const u16 vi = i == 0 ? p_cim[0] : i == 1 ? p_cim[1] : p_cim[2];
                const float ar = -rbf(expf(bf_lo(vl))) * dt * 1.44269504088896341f;
                const float rv = bf_lo(va) * dt * 0.15915494309189535f;
                const f2 z1 = cexp_rev(ar, rv), z8 = cexp_rev(8.f * ar, 8.f * rv), zb = cexp_rev(256.f * ar, 256.f * rv);
                const f2 z1s = {-z1.y, z1.x}, z8s = {-z8.y, z8.x}, zbs = {-zb.y, zb.x};
                u32* tx = (u32*)(tab + lane * 4);
                u32* ty = (u32*)(tab + 27 * 272 + lane * 4);
#define PUTX(r, v) tx[(r) * 68] = pk_h((v).x, -(v).y)
                f2 xa[9];
                xa[0] = f2{__builtin_amdgcn_ldexpf(bf_lo(vr), 7 - ce), __builtin_amdgcn_ldexpf(bf_lo(vi), 7 - ce)};
#pragma unroll
                for (int j = 1; j < 9; ++j) xa[j] = cmul(xa[j - 1], z1, z1s);
#pragma unroll
                for (int j = 0; j < 8; ++j) PUTX(j, xa[j]);
                f2 a0 = cmul(xa[0], zb, zbs), a2 = cmul(xa[2], zb, zbs), a4 = cmul(xa[4], zb, zbs), a6 = cmul(xa[6], zb, zbs), a8 = cmul(xa[8], zb, zbs);
                PUTX(8, a0); PUTX(9, a2); PUTX(10, a4); PUTX(11, a6); PUTX(16, a8);
                a0 = cmul(a0, zb, zbs); a4 = cmul(a4, zb, zbs); a8 = cmul(a8, zb, zbs);
                PUTX(17, a0); PUTX(18, a4); PUTX(19, a8);
                a0 = cmul(a0, zb, zbs); a4 = cmul(a4, zb, zbs); a8 = cmul(a8, zb, zbs);
                PUTX(24, a0); PUTX(25, a4); PUTX(26, a8);
                a0 = cmul(a0, zb, zbs); a8 = cmul(a8, zb, zbs); PUTX(12, a0); PUTX(13, a8);
                a0 = cmul(a0, zb, zbs); a8 = cmul(a8, zb, zbs); PUTX(14, a0); PUTX(15, a8);
                a0 = cmul(a0, zb, zbs); a8 = cmul(a8, zb, zbs); PUTX(20, a0); PUTX(21, a8);
                a0 = cmul(a0, zb, zbs); a8 = cmul(a8, zb, zbs); PUTX(22, a0); PUTX(23, a8);
                const f2 z16 = cmul(z8, z8, z8s), z16s = {-z16.y, z16.x};
                f2 ye = z16, yo = z8;
                ty[0] = pk_h(1.f, 0.f); ty[68] = pk_h(yo.x, yo.y);
                ty[2 * 68] = pk_h(ye.x, ye.y);
                yo = cmul(yo, z16, z16s); ty[3 * 68] = pk_h(yo.x, yo.y);
#pragma unroll
                for (int j = 4; j < 32; j += 2) {
                    ye = cmul(ye, z16, z16s); yo = cmul(yo, z16, z16s);
                    ty[j * 68] = pk_h(ye.x, ye.y); ty[(j + 1) * 68] = pk_h(yo.x, yo.y);
                }
                __builtin_amdgcn_wave_barrier();
                const char* fa = tab + (lane & 31) * 272 + (lane >> 5) * 16;
                h8 fA[8], fB[8];
#pragma unroll
                for (int ks = 0; ks < 8; ++ks) { fA[ks] = *(const h8*)(fa + ks * 32); fB[ks] = *(const h8*)(fa + 27 * 272 + ks * 32); }
                __builtin_amdgcn_sched_barrier(0);
#pragma unroll
                for (int ks = 0; ks < 8; ++ks) kacc = __builtin_amdgcn_mfma_f32_32x32x16_f16(fA[ks], fB[ks], kacc, 0, 0, 0);
                __builtin_amdgcn_wave_barrier();
            }
            STAMP(2);
            const int j = lane & 31, h = lane >> 5;
            char* kb = kf + 16;
            *(u2*)(kb + (8 * j + 4 * h) * 2) = u2{pk_h(kacc[0], kacc[1]), pk_h(kacc[2], kacc[3])};
            if (lane == 0) *(u4*)kf = u4{0, 0, 0, 0};
            if (h == 0) {
                *(u4*)(kb + (256 + 8 * j) * 2) = u4{pk_h(kacc[4], kacc[4]), pk_h(kacc[5], kacc[6]), pk_h(kacc[6], kacc[6]), pk_h(kacc[7], kacc[8])};
                *(u4*)(kb + (512 + 8 * j) * 2) = u4{pk_h(kacc[9], kacc[9]), pk_h(kacc[9], kacc[10]), pk_h(kacc[10], kacc[10]), pk_h(kacc[11], kacc[11])};
                *(u4*)(kb + (768 + 8 * j) * 2) = u4{pk_h(kacc[12], kacc[12]), pk_h(kacc[12], kacc[13]), pk_h(kacc[13], kacc[13]), pk_h(kacc[14], kacc[14])};
            } else {
#pragma unroll
                for (int q = 0; q < 4; ++q) {
                    const float P = kacc[4 + 2 * q], N = kacc[5 + 2 * q];
                    const u32 pp = pk_h(P, P), nn = pk_h(N, N);
                    *(u4*)(kb + (1024 + 256 * q + 8 * j) * 2) = u4{pp, pp, pk_h((j & 1) ? N : P, N), nn};
                }
            }
            __builtin_amdgcn_wave_barrier();
            if (lane < 8) *(u4*)(tab + lane * 16) = u4{0, 0, 0, 0};
            u32 kw[16][3];
#pragma unroll
            for (int it = 0; it < 16; ++it) {
                const u32* src = (const u32*)(kb + (lane + 64 * it) * 4);
                kw[it][0] = src[-2]; kw[it][1] = src[-1]; kw[it][2] = src[0];
            }
            __builtin_amdgcn_sched_barrier(0);
#pragma unroll
            for (int it = 0; it < 16; ++it) {
                u4 o;
                o[0] = __builtin_amdgcn_perm(kw[it][1], kw[it][2], 0x07060100);
                o[1] = __builtin_amdgcn_perm(kw[it][0], kw[it][1], 0x07060100);
                o[2] = __builtin_amdgcn_alignbit(kw[it][2], kw[it][2], 16);
                o[3] = __builtin_amdgcn_alignbit(kw[it][1], kw[it][1], 16);
                *(u4*)(tab + 128 + (lane + 64 * it) * 16) = o;
            }
            STAMP(3);
        } else {
#pragma unroll
            for (int q = 0; q < 4; ++q)
#pragma unroll
                for (int e = 0; e < 2; ++e) xr[q][e] = *(const u2*)(x + (size_t)(2 * (t + 256 * q) + e) * D + grp * 4);
            dskv = *(const u2*)(dsk + grp * 4);
            if (t < 128) *(u4*)(smem + CV_U0 + (t >> 5) * CV_U + (t & 31) * 16) = u4{0, 0, 0, 0};
            u4 st[4];
            for (u32 spin = 0; spin < SPIN_CAP; ++spin) {
            asm volatile("" ::: "memory");
                bool ok = true;
#pragma unroll
                for (int q = 0; q < 4; ++q) { st[q] = ld16(wsr, WS_STATS + (t + 256 * q) * 16); ok = ok && st[q][1] == tagv && st[q][3] == tagv; }
                if (__all(ok)) break;
                __builtin_amdgcn_s_sleep(2);
            }
            STAMP(1);
#pragma unroll
            for (int q = 0; q < 4; ++q) {
                const f2 m = {bf_lo(st[q][0]), bf_lo(st[q][2])};
                const f2 r = {__builtin_amdgcn_rcpf(bf_hi(st[q][0])), __builtin_amdgcn_rcpf(bf_hi(st[q][2]))};
#pragma unroll
                for (int c = 0; c < 4; ++c) {
                    const f2 xv = (c & 1) ? f2{bf_hi(xr[q][0][c >> 1]), bf_hi(xr[q][1][c >> 1])} : f2{bf_lo(xr[q][0][c >> 1]), bf_lo(xr[q][1][c >> 1])};
                    const u32 uu = pk_bf(un_bf(pk_bf(xv - m)) * r);
                    ub[q][c] = uu;
                    *(u32*)(smem + CV_U0 + c * CV_U + 512 + (t + 256 * q) * 4) = pk_h(bf_lo(uu), bf_hi(uu));
                }
            }
            STAMP(2);
        }
        BAR();
        STAMP(4);
        if (w < 4) {
            const int r = lane & 15, g = lane >> 4;
            const char* pa = smem + CV_TAB0 + w * CV_TAB + (r + 32 - 8 * g) * 8;
            const char* pb = smem + CV_U0 + w * CV_U + 512 + (16 * r - 16 + 8 * g) * 2;
            h8 A[2][8], B[2][8];
            auto ldf = [&](int om, int buf) {
#pragma unroll
                for (int a = 0; a < 8; ++a) {
                    const u2 lo = *(const volatile LAS u2*)(LAS char*)(pa + (8 * a + om) * 256);
                    const u2 hi = *(const volatile LAS u2*)(LAS char*)(pa + (8 * a + om) * 256 - 32);
                    A[buf][a] = __builtin_bit_cast(h8, u4{lo[0], lo[1], hi[0], hi[1]});
                    B[buf][a] = *(const h8*)(pb + a * 512 - om * 64);
                }
            };
            ldf(0, 0);
            __builtin_amdgcn_sched_barrier(0);
#pragma unroll
            for (int om = 0; om < 8; ++om) {
                if (om < 7) ldf(om + 1, (om + 1) & 1);
#pragma unroll
                for (int a = 0; a < 8; ++a)
#pragma unroll
                    for (int i = a; i < 8; ++i) acc[i] = __builtin_amdgcn_mfma_f32_16x16x32_f16(A[om & 1][a], B[om & 1][i - a], acc[i], 0, 0, 0);
                if (om < 7) {
#pragma unroll
                    for (int k = 0; k < 24; ++k) { __builtin_amdgcn_sched_group_barrier(0x8, 1, 0); __builtin_amdgcn_sched_group_barrier(0x100, 1, 0); }
                    __builtin_amdgcn_sched_group_barrier(0x8, 12, 0);
                }
                __builtin_amdgcn_sched_barrier(0);
            }
            char* ys = smem + CV_KF0 + w * CV_KF;
#pragma unroll
            for (int i = 0; i < 8; ++i)
                *(u2*)(ys + (256 * i + 16 * r + 4 * g) * 2) = u2{pk_bf(acc[i][0] * yscale, acc[i][1] * yscale), pk_bf(acc[i][2] * yscale, acc[i][3] * yscale)};
        } else {
            stage_w13(w1, jj, w & 3, lds0, lane);
        }
        STAMP(5);
        BAR();
        if (w < 4) {
            stage_w13(w3, jj, w, lds0 + G1_IMG3, lane);
        } else {
            const f2 ds01 = un_bf(dskv[0]), ds23 = un_bf(dskv[1]);
#pragma unroll
            for (int q = 0; q < 4; ++q) {
                f2 hv[4];
#pragma unroll
                for (int c = 0; c < 4; ++c) {
                    const u32 y = *(const u32*)(smem + CV_KF0 + c * CV_KF + (t + 256 * q) * 4);
                    const float dsc = c == 0 ? ds01.x : c == 1 ? ds01.y : c == 2 ? ds23.x : ds23.y;
                    const u32 sk = pk_bf(un_bf(ub[q][c]) * dsc);
                    const u32 tt = pk_bf(un_bf(y) + un_bf(sk));
                    const f2 xv = (c & 1) ? f2{bf_hi(xr[q][0][c >> 1]), bf_hi(xr[q][1][c >> 1])} : f2{bf_lo(xr[q][0][c >> 1]), bf_lo(xr[q][1][c >> 1])};
                    hv[c] = xv + un_bf(tt);
                }
                st16(wsr, WS_H4 + grp * (L * 8) + (t + 256 * q) * 16,
                     u4{pk_bf(hv[0].x, hv[1].x), pk_bf(hv[2].x, hv[3].x), pk_bf(hv[0].y, hv[1].y), pk_bf(hv[2].y, hv[3].y)});
            }
            STAMP(6);
            WAITV(0);
        }
        BAR();
        if (tid == 0) st4(wsr, WS_FLAG1 + grp * 16, tagv);
        STAMP(7);
    }

    if (w == 0) {
        for (u32 spin = 0; spin < SPIN_CAP; ++spin) {
            asm volatile("" ::: "memory");
            const u32 a = ld4(wsr, WS_FLAG1 + lane * 16), b = ld4(wsr, WS_FLAG1 + (lane + 64) * 16), c = ld4(wsr, WS_FLAG1 + (128 + (lane & 31)) * 16);
            if (__all(a == tagv && b == tagv && c == tagv)) break;
            __builtin_amdgcn_s_sleep(1);
        }
        if (blockIdx.x == 0 && lane == 0) st4(wsr, WS_EPOCH, tagv);
    } else if (w == 1 || w == 2) {
        const int c = lane < 48 ? lane : 47;
        u2 g;
        for (u32 spin = 0; spin < SPIN_CAP; ++spin) {
            asm volatile("" ::: "memory");
            g = ld8(wsr, WS_CS + ((w - 1) * F + jj * 48 + c) * 8);
            if (__all(g[1] == tagv)) break;
            __builtin_amdgcn_s_sleep(1);
        }
        ((u32*)(smem + G1_CS))[(w - 1) * 48 + c] = g[0];
    }
    WAITV(0);
    BAR();
    STAMP(8);
    {
        const int tok0 = xcd * 256 + w * 32;
        const int hoff = WS_H4 + (lane >> 4) * (L * 8) + (tok0 + 2 * (lane & 15)) * 8;
        u4 tk[40];
#pragma unroll
        for (int s = 0; s < 40; ++s) tk[s] = __builtin_amdgcn_raw_buffer_load_b128(wsr, hoff, s * 4 * (L * 8), SC1);
        f16v acc[3] = {};
        float ssum = 0.f, ssq = 0.f;
        const int p = lane & 3, pp = ((p & 1) << 1) | (p >> 1);
        const char* fb = smem + ((lane >> 4) & 1) * G1_IMG3 + (8 * (lane >> 5) + ((lane & 15) >> 2)) * 96 + pp * 8;
        b8 wf[2][3];
        auto ldw = [&](int s, int buf) {
#pragma unroll
            for (int T = 0; T < 3; ++T) { const char* q = fb + s * 16 * 96 + T * 32; wf[buf][T] = cat(trd(q), trd(q + 4 * 96)); }
        };
        ldw(0, 0);
        __builtin_amdgcn_sched_barrier(0);
#pragma unroll
        for (int s = 0; s < 40; ++s) {
            if (s < 39) ldw(s + 1, (s + 1) & 1);
            u4 t = tk[s];
            auto s0 = __builtin_amdgcn_permlane16_swap(t[0], t[2], false, false);
            auto s1 = __builtin_amdgcn_permlane16_swap(t[1], t[3], false, false);
            t = u4{s0[0], s1[0], s0[1], s1[1]};
            const b8 bt = __builtin_bit_cast(b8, t);
#pragma unroll
            for (int T = 0; T < 3; ++T) acc[T] = __builtin_amdgcn_mfma_f32_32x32x16_bf16(wf[s & 1][T], bt, acc[T], 0, 0, 0);
#pragma unroll
            for (int j = 0; j < 4; ++j) { ssum = dot2(t[j], 0x3f803f80u, ssum); ssq = dot2(t[j], t[j], ssq); }
            if (s < 39) __builtin_amdgcn_sched_group_barrier(0x100, 6, 0);
            __builtin_amdgcn_sched_barrier(0);
        }
        STAMP(9);
        ssum += __shfl_xor(ssum, 32); ssq += __shfl_xor(ssq, 32);
        const u32 stp = ln_pack(ssum, ssq);
        const float nmean = -bf_lo(stp), sd = bf_hi(stp);
        const float ce = -1.44269504088896341f * __builtin_amdgcn_rcpf(sd), cv = sd * sd;
        const int h = lane >> 5;
        const int tok = tok0 + 2 * (lane & 15) + ((lane >> 4) & 1);
        const int ooff = WS_HB + ((jj * 6 + h) * L + tok) * 16;
#pragma unroll
        for (int T = 0; T < 3; ++T) {
            const float* cg = (const float*)(smem + G1_CS) + 16 * T + 8 * h;
            const f4 g0 = *(const f4*)cg, g1 = *(const f4*)(cg + 4), u0 = *(const f4*)(cg + 48), u1 = *(const f4*)(cg + 52);
            u4 o;
#pragma unroll
            for (int j = 0; j < 4; ++j) {
                const f2 cgj = j == 0 ? g0.xy : j == 1 ? g0.zw : j == 2 ? g1.xy : g1.zw;
                const f2 cuj = j == 0 ? u0.xy : j == 1 ? u0.zw : j == 2 ? u1.xy : u1.zw;
                const f2 G = f2{acc[T][2 * j], acc[T][2 * j + 1]} + cgj * nmean;
                const f2 U = f2{acc[T][8 + 2 * j], acc[T][9 + 2 * j]} + cuj * nmean;
                const f2 e = G * ce;
                const f2 den = f2{__builtin_amdgcn_exp2f(e.x), __builtin_amdgcn_exp2f(e.y)} * cv + cv;
                o[j] = pk_bf(G * U * f2{__builtin_amdgcn_rcpf(den.x), __builtin_amdgcn_rcpf(den.y)});
            }
            st16(wsr, ooff + T * 2 * L * 16, o);
        }
        STAMP(10);
        WAITV(0);
        BAR();
        if (tid == 0) st4(wsr, WS_FLAG2 + (xcd * 32 + jj) * 16, tagv);
        STAMP(11);
    }

    {
        const int mt = xcd * 4 + (jj & 3), nt = jj >> 2;
        const int th = w & 1, kq = w >> 1;
        const char* wsrc = uni((const char*)w2 + nt * 160 + (size_t)(kq * 384) * (D * 2));
        u32 vo[3];
#pragma unroll
        for (int u = 0; u < 3; ++u) {
            const int pc = min((th * 3 + u) * 64 + lane, 319), r = pc / 10;
            const int kr = (r & ~12) | ((r & 4) << 1) | ((r & 8) >> 1);
            vo[u] = kr * (D * 2) + (pc % 10) * 16;
        }
        auto stage = [&](int c) {
            const u32 dst = lds0 + (c % G2_NSLOT) * G2_SLOT + kq * 5120 + th * 3072;
            glds16(wsrc + (size_t)c * 32 * (D * 2), vo[0], dst);
            glds16(wsrc + (size_t)c * 32 * (D * 2), vo[1], dst + 1024);
            if (th == 0) glds16(wsrc + (size_t)c * 32 * (D * 2), vo[2], dst + 2048);
        };
#pragma unroll
        for (int c = 0; c < 7; ++c) stage(c);
        if (w == 0) {
            for (u32 spin = 0; spin < SPIN_CAP; ++spin) {
            asm volatile("" ::: "memory");
                const u32 a = ld4(wsr, WS_FLAG2 + (xcd * 32 + (lane & 31)) * 16);
                if (__all(a == tagv)) break;
                __builtin_amdgcn_s_sleep(1);
            }
        }
        WAITV(0);
        BAR();
        STAMP(12);
        const int tok0 = mt * 64 + th * 32;
        const int hoff = WS_HB + ((kq * 48 + (lane >> 4)) * L + tok0 + (lane & 15)) * 16;
        u4 tk[12][2];
#pragma unroll
        for (int s = 0; s < 12; ++s)
#pragma unroll
            for (int e = 0; e < 2; ++e) tk[s][e] = __builtin_amdgcn_raw_buffer_load_b128(wsr, hoff + e * 256, s * 4 * L * 16, SC1);
        const int g = lane >> 4;
        const char* fb = smem + kq * 5120 + (16 * (g >> 1) + 4 * (g & 1) + ((lane & 15) >> 2)) * 160 + (lane & 3) * 8;
        b8 wf[5][5];
        auto ldw = [&](int s, int buf) {
#pragma unroll
            for (int T = 0; T < 5; ++T) { const char* q = fb + (s % G2_NSLOT) * G2_SLOT + T * 32; wf[buf][T] = cat(trd(q), trd(q + 8 * 160)); }
        };
#pragma unroll
        for (int s = 0; s < 5; ++s) ldw(s, s);
        BAR();
#pragma unroll
        for (int c = 7; c < 12; ++c) stage(c);
        f4 acc[5][2] = {};
#pragma unroll
        for (int s = 0; s < 12; ++s) {
            if (s == 5) { WAITV(0); BAR(); }
            if (s >= 3 && s + 2 < 12 && (s + 2 < 7 || s >= 5)) ldw(s + 2, (s + 2) % 5);
#pragma unroll
            for (int T = 0; T < 5; ++T)
#pragma unroll
                for (int e = 0; e < 2; ++e) acc[T][e] = __builtin_amdgcn_mfma_f32_16x16x32_bf16(wf[s % 5][T], __builtin_bit_cast(b8, tk[s][e]), acc[T][e], 0, 0, 0);
            __builtin_amdgcn_sched_barrier(0);
        }
        STAMP(13);
        const int tok = tok0 + 16 * (kq & 1) + (lane & 15);
        u2 hres[5];
        if (kq >= 2) {
#pragma unroll
            for (int T = 0; T < 5; ++T) hres[T] = ld8(wsr, WS_H4 + ((nt * 20 + 4 * T + g) * L + tok) * 8);
        }
        BAR();
        f4* red = (f4*)smem + lane;
        if (kq < 2) {
#pragma unroll
            for (int T = 0; T < 5; ++T) { red[((w * 2 + 0) * 5 + T) * 64] = acc[T][0]; red[((w * 2 + 1) * 5 + T) * 64] = acc[T][1]; }
        } else {
#pragma unroll
            for (int T = 0; T < 5; ++T) {
                const f4 a0 = acc[T][0], a1 = acc[T][1];
                if (kq & 1) { acc[T][0] = a1; acc[T][1] = a0; }
                red[((w * 2) * 5 + T) * 64] = acc[T][1];
            }
        }
        BAR();
        if (kq >= 2) {
            const int e = kq & 1;
#pragma unroll
            for (int T = 0; T < 5; ++T) {
                const f4 o = acc[T][0] + red[(((w ^ 2) * 2) * 5 + T) * 64] + red[((th * 2 + e) * 5 + T) * 64] + red[(((2 + th) * 2 + e) * 5 + T) * 64];
                const u32 a = pk_bf(o[0], o[1]), b = pk_bf(o[2], o[3]);
                *(u2*)(out + (size_t)tok * D + nt * 80 + 16 * T + 4 * g) = u2{pk_bf(un_bf(hres[T][0]) + un_bf(a)), pk_bf(un_bf(hres[T][1]) + un_bf(b))};
            }
        }
        STAMP(14);
    }
}

extern "C" void kernel_launch(void* const* d_in, const int* in_sizes, int n_in, void* d_out, int out_size, void* d_ws, size_t ws_size, hipStream_t stream) {
    (void)hipFuncSetAttribute((const void*)k_all, hipFuncAttributeMaxDynamicSharedMemorySize, LDS_BYTES);
    k_all<<<256, 512, LDS_BYTES, stream>>>((const u16*)d_in[0], (const u16*)d_in[1], (const u16*)d_in[2], (const u16*)d_in[3], (const u16*)d_in[4],
                                           (const u16*)d_in[5], (const u16*)d_in[6], (const u16*)d_in[8], (const u16*)d_in[9], (const u16*)d_in[10],
                                           (char*)d_ws, (u16*)d_out);
}
```

```cpp
#include <hip/hip_runtime.h>
#include <stdint.h>
#include <type_traits>

typedef unsigned short u16;
typedef unsigned int u32;
typedef float f2 __attribute__((ext_vector_type(2)));
typedef float f4 __attribute__((ext_vector_type(4)));
typedef float f16v __attribute__((ext_vector_type(16)));
typedef _Float16 h2 __attribute__((ext_vector_type(2)));
typedef _Float16 h8 __attribute__((ext_vector_type(8)));
typedef __bf16 b2 __attribute__((ext_vector_type(2)));
typedef __bf16 b8 __attribute__((ext_vector_type(8)));
typedef short s4 __attribute__((ext_vector_type(4)));
typedef u32 u2 __attribute__((ext_vector_type(2)));
typedef u32 u4 __attribute__((ext_vector_type(4)));
typedef __amdgpu_buffer_rsrc_t rsrc_t;
#define LAS __attribute__((address_space(3)))
#define DEVI __device__ __forceinline__

constexpr int L = 2048, D = 640, NS = 192, F = 1536;

#define FSV 32
#define PSLEEP 0
constexpr int FS = FSV;
constexpr int WS_EPOCH = 0;
constexpr int WS_STAT2 = 16384;
constexpr int WS_FLAG2 = 65536;
constexpr int WS_REP   = 131072, REPS = 49152, F1H = 10240;
constexpr int RP_STATS = 0;
constexpr int RP_FLAG1 = 16384;
constexpr int RP_PACK  = 36864;
constexpr int WS_CS    = 524288;
constexpr int WS_H4    = 1048576;
constexpr int HS = 40960 + 512, HBS = 49152 + 512;
constexpr int WS_HB    = WS_H4 + 64 * HS;
constexpr int P2S = 245760 + 512;
constexpr int WS_P2    = WS_HB + 128 * HBS;
constexpr int WS_END   = WS_P2 + 8 * P2S;
constexpr size_t WS_TS = 32u << 20;
constexpr int SC1 = 16;
constexpr u32 SPIN_CAP = 1u << 14;

constexpr int LDS_BYTES = 163840;
constexpr int LDS_CNT = LDS_BYTES - 256;
constexpr int G1_IMG1 = LDS_CNT - 123008;
constexpr int G1_CS   = 4096;
constexpr int CV_R = 8256, CV_U = 4608, CV_Y = 32 * 272;
constexpr int CV_U0 = 4 * CV_R;
constexpr int CV_Y0 = CV_U0 + 4 * CV_U;
constexpr int CV_Y1 = G1_IMG1 + 128 * 96;
static_assert(CV_Y0 <= CV_Y1 && CV_Y0 + 4 * CV_Y <= G1_IMG1 + 480 * 96 && G1_IMG1 % 16 == 0, "lds map");
enum { cK, cU1, cU2, cY1, cY2, cW, cD1A, cD1B, fA, fB, cCS, cL, cD2A, cD2B, fA2, fB2, cS, cP = cS + 4, cRf = cP + 4, cRedA = cRf + 4, cRedB, cEd, cPk, cPart, cP13, cG1, cR2, cSt };

DEVI float bf_lo(u32 v) { return __uint_as_float(v << 16); }
DEVI float bf_hi(u32 v) { return __uint_as_float(v & 0xffff0000u); }
DEVI u32 pk_bf(float a, float b) { b2 r = {(__bf16)a, (__bf16)b}; return __builtin_bit_cast(u32, r); }
DEVI u32 pk_bf(f2 a) { return pk_bf(a.x, a.y); }
DEVI f2 un_bf(u32 v) { return f2{bf_lo(v), bf_hi(v)}; }
DEVI u32 pk_h(float a, float b) { h2 r = {(_Float16)a, (_Float16)b}; return __builtin_bit_cast(u32, r); }
DEVI float rbf(float a) { return bf_lo(pk_bf(a, a)); }
DEVI float dot2(u32 a, u32 b, float c) { return __builtin_amdgcn_fdot2_f32_bf16(__builtin_bit_cast(b2, a), __builtin_bit_cast(b2, b), c, false); }
template <int CTRL> DEVI float dppf(float v) { return __uint_as_float(__builtin_amdgcn_update_dpp(0u, __float_as_uint(v), CTRL, 0xf, 0xf, true)); }
template <int CTRL> DEVI float dppo(float old, float v) { return __uint_as_float(__builtin_amdgcn_update_dpp(__float_as_uint(old), __float_as_uint(v), CTRL, 0xf, 0xf, false)); }
DEVI float opq(float r) { asm("" : "+v"(r)); return r; }
DEVI float row_sum16(float v) { v = opq(v + dppf<0x128>(v)); v = opq(v + dppf<0x124>(v)); v = opq(v + dppf<0x122>(v)); v = opq(v + dppf<0x121>(v)); return v; }
DEVI float rl(float v, int l) { return __uint_as_float(__builtin_amdgcn_readlane(__float_as_uint(v), l)); }

#define STAMP(i)
#define TST(i)
#define KST(i)
#define SB() __builtin_amdgcn_sched_barrier(0)

DEVI int lane_id() { int l; asm volatile("v_mbcnt_lo_u32_b32 %0, -1, 0\n\tv_mbcnt_hi_u32_b32 %0, -1, %0" : "=v"(l)); return l; }
DEVI u32 ln_pack(float sum, float sq) {
    float mean = sum * (1.f / D), msq = sq * (1.f / D);
    asm("" : "+v"(mean));
    const float var = fmaxf(msq - mean * mean, 0.f);
    const float sd = __builtin_amdgcn_sqrtf(rbf(rbf(var) + 1e-5f));
    return pk_bf(mean, sd);
}
DEVI void glds16(const void* sbase, u32 voff, u32 lds_dst) {
    asm volatile("s_mov_b32 m0, %2\n\ts_nop 0\n\tglobal_load_lds_dwordx4 %0, %1" :: "v"(voff), "s"(sbase), "s"(lds_dst) : "memory");
}
DEVI const char* uni(const void* p) {
    const u32 lo = __builtin_amdgcn_readfirstlane((u32)(size_t)p), hi = __builtin_amdgcn_readfirstlane((u32)((size_t)p >> 32));
    size_t r = ((size_t)hi << 32) | lo;
    asm volatile("s_nop 4" : "+s"(r));
    return (const char*)r;
}
#define WAITV(n) asm volatile("s_waitcnt vmcnt(" #n ")" ::: "memory")
#define BAR() do { asm volatile("s_waitcnt lgkmcnt(0)" ::: "memory"); __builtin_amdgcn_s_barrier(); asm volatile("" ::: "memory"); } while (0)
#define POLL _Pragma("nounroll") for (u32 spin = 0; spin < SPIN_CAP; ++spin)
#define FENCE() asm volatile("" ::: "memory")
DEVI s4 trd(const char* p) { return __builtin_amdgcn_ds_read_tr16_b64_v4i16((LAS s4*)p); }
DEVI b8 cat(s4 a, s4 b) { return __builtin_bit_cast(b8, __builtin_shufflevector(a, b, 0, 1, 2, 3, 4, 5, 6, 7)); }
DEVI u4 ld16(rsrc_t r, int off, int so = 0) { return __builtin_amdgcn_raw_buffer_load_b128(r, off, so, SC1); }
DEVI u2 ld8(rsrc_t r, int off) { return __builtin_amdgcn_raw_buffer_load_b64(r, off, 0, SC1); }
DEVI u32 ld4(rsrc_t r, int off) { return __builtin_amdgcn_raw_buffer_load_b32(r, off, 0, SC1); }
DEVI void st16(rsrc_t r, int off, u4 v) { __builtin_amdgcn_raw_buffer_store_b128(v, r, off, 0, SC1); }
DEVI void st8(rsrc_t r, int off, u2 v) { __builtin_amdgcn_raw_buffer_store_b64(v, r, off, 0, SC1); }
DEVI void st4(rsrc_t r, int off, u32 v) { __builtin_amdgcn_raw_buffer_store_b32(v, r, off, 0, SC1); }

DEVI float addS(float a, float b) { return opq(a + b); }
DEVI float subS(float a, float b) { return opq(a - b); }
DEVI float mulS(float a, float b) { return opq(a * b); }
DEVI float fmaS(float a, float b, float c) { return opq(__builtin_fmaf(a, b, c)); }
DEVI f2 cmul(f2 a, f2 z, f2 zs) { return a.xx * z + a.yy * zs; }
DEVI f2 cexp_rev(float lg2, float rev) {
    const float m = __builtin_amdgcn_exp2f(lg2), r = __builtin_amdgcn_fractf(rev);
    return f2{__builtin_amdgcn_cosf(r), __builtin_amdgcn_sinf(r)} * m;
}
DEVI float exp_acc(float x) {
    const float hi = x * 1.44269504088896341f;
    const float lo = __builtin_fmaf(x, 1.44269504088896341f, -hi) + x * 1.92596299112661746e-8f;
    const float e = __builtin_amdgcn_exp2f(hi);
    return __builtin_fmaf(e, lo * 0.693147180559945309f, e);
}
template <int R0, int R1, int R2, int R3, int R4, int R5, int R6, int R7>
DEVI void put8(u32 base, u32 v0, u32 v1, u32 v2, u32 v3, u32 v4, u32 v5, u32 v6, u32 v7) {
    asm volatile("s_mov_b32 m0, %16\n\ts_nop 0\n\tds_write_addtid_b32 %0 offset:%8\n\tds_write_addtid_b32 %1 offset:%9\n\tds_write_addtid_b32 %2 offset:%10\n\tds_write_addtid_b32 %3 offset:%11\n\t"
                 "ds_write_addtid_b32 %4 offset:%12\n\tds_write_addtid_b32 %5 offset:%13\n\tds_write_addtid_b32 %6 offset:%14\n\tds_write_addtid_b32 %7 offset:%15"
                 :: "v"(v0), "v"(v1), "v"(v2), "v"(v3), "v"(v4), "v"(v5), "v"(v6), "v"(v7), "i"(R0), "i"(R1), "i"(R2), "i"(R3), "i"(R4), "i"(R5), "i"(R6), "i"(R7), "s"(base) : "memory");
}
DEVI u32 cnt_addr(char* smem, int c) { return (u32)(size_t)(LAS char*)smem + LDS_CNT + 4 * c; }
DEVI void sig(char* smem, int c, int) {
    asm volatile("s_mov_b64 exec, 1\n\tds_add_u32 %0, %1\n\ts_mov_b64 exec, -1" :: "v"(cnt_addr(smem, c)), "v"(1u) : "memory");
}
DEVI u32 sig_ret(char* smem, int c, int) {
    u32 t, r;
    asm volatile("s_mov_b64 exec, 1\n\tds_add_rtn_u32 %0, %2, %3\n\ts_waitcnt lgkmcnt(0)\n\tv_readfirstlane_b32 %1, %0\n\ts_mov_b64 exec, -1"
                 : "=&v"(t), "=s"(r) : "v"(cnt_addr(smem, c)), "v"(1u) : "memory");
    return r;
}
DEVI u32 peek(char* smem, int c) {
    u32 t, r;
    asm volatile("ds_read_b32 %0, %2\n\ts_waitcnt lgkmcnt(0)\n\tv_readfirstlane_b32 %1, %0" : "=&v"(t), "=s"(r) : "v"(cnt_addr(smem, c)) : "memory");
    return r;
}
DEVI void await(char* smem, int c, u32 n) {
    u32 t, r, k;
    asm volatile("s_mov_b32 %2, 0x100000\n"
                 ".Law%=:\n\tds_read_b32 %0, %3\n\ts_waitcnt lgkmcnt(0)\n\tv_readfirstlane_b32 %1, %0\n\ts_cmp_ge_u32 %1, %4\n\ts_cbranch_scc1 .Lae%=\n\t"
                 "s_sleep 1\n\ts_sub_u32 %2, %2, 1\n\ts_cbranch_scc0 .Law%=\n"
                 ".Lae%=:" : "=&v"(t), "=&s"(r), "=&s"(k) : "v"(cnt_addr(smem, c)), "s"(n) : "memory", "scc");
}
DEVI void awaitf(char* smem, int c, u32 n) {
    u32 t, r, k;
    asm volatile("s_mov_b32 %2, 0x400000\n"
                 ".Law%=:\n\tds_read_b32 %0, %3\n\ts_waitcnt lgkmcnt(0)\n\tv_readfirstlane_b32 %1, %0\n\ts_cmp_ge_u32 %1, %4\n\ts_cbranch_scc1 .Lae%=\n\t"
                 "s_sub_u32 %2, %2, 1\n\ts_cbranch_scc0 .Law%=\n"
                 ".Lae%=:" : "=&v"(t), "=&s"(r), "=&s"(k) : "v"(cnt_addr(smem, c)), "s"(n) : "memory", "scc");
}
DEVI void pollN(rsrc_t wsr, int base, int stride, int n, int first, u32 tagv, int lane) {
    if (first >= 0) {
    POLL {
        FENCE();
        if (__all(ld4(wsr, base + first * stride) == tagv)) break;
        __builtin_amdgcn_s_sleep(2);
    }
    }
    bool n0 = lane < n, n1 = lane + 64 < n, n2 = lane + 128 < n;
    POLL {
        FENCE();
        const u32 a = ld4(wsr, n0 ? base + lane * stride : WS_END), b = ld4(wsr, n1 ? base + (lane + 64) * stride : WS_END), c = ld4(wsr, n2 ? base + (lane + 128) * stride : WS_END);
        n0 = n0 && a != tagv; n1 = n1 && b != tagv; n2 = n2 && c != tagv;
        if (!__any(n0 || n1 || n2)) break;
        if (PSLEEP) __builtin_amdgcn_s_sleep(PSLEEP);
    }
    FENCE();
}
template <int NP, int N, int LPP> DEVI void watch_touch(rsrc_t wsr, int flag0, int stride, const int (&a)[N], u32 tagv, int lane) {
    u32 done = 0, pf = 0;
    POLL {
        FENCE();
        const u32 up = (u32)__ballot(ld4(wsr, flag0 + min(lane, NP - 1) * stride) == tagv) & ((1u << NP) - 1u);
        const u32 nw = up & ~done;
        if (nw) {
#pragma unroll
            for (int k = 0; k < N; ++k) pf ^= ld4(wsr, ((nw >> ((lane + 64 * k) / LPP)) & 1u) ? a[k] : WS_END);
            done = up;
            if (done == (1u << NP) - 1u) break;
        } else __builtin_amdgcn_s_sleep(2);
    }
    asm volatile("" :: "v"(pf));
}
DEVI void touch_h(rsrc_t wsr, int flags, int hbase, int jj, u32 tagv, int lane) {
    int a[3];
#pragma unroll
    for (int k = 0; k < 3; ++k) { const int l = lane + 64 * k; a[k] = hbase + ((l & 31) >> 1) * HS + (jj * 5 + (l >> 5)) * 256 + (l & 1) * 128; }
    watch_touch<5, 3, 32>(wsr, flags + jj * 5 * FS, FS, a, tagv, lane);
}
DEVI void stage_w13(const u16* w1, const u16* w3, int ub0, int ubs, int g0, int g1, int gs, u32 lds0, int lane) {
    const char* s1 = uni((const char*)w1 + ub0 * 32);
    const char* s3 = uni((const char*)w3 + ub0 * 32);
    u32 vo[3];
#pragma unroll
    for (int b = 0; b < 3; ++b) { const int pc = b * 64 + lane; vo[b] = (pc / 6) * (F * 2) + ((pc % 6) >> 1) * ubs * 32 + (pc & 1) * 16; }
#pragma unroll 1
    for (int g = g0; gs > 0 ? g < g1 : g > g1; g += gs) {
        const char* src = (g < 20 ? s1 : s3 - 20 * 32 * (F * 2)) + g * 32 * (F * 2);
        const u32 dst = lds0 + G1_IMG1 + g * 3072 + (g < 20 ? 0 : 128);
#pragma unroll
        for (int b = 0; b < 3; ++b) glds16(src, vo[b], dst + b * 1024);
    }
}
DEVI void ln2_load(u4 (&sv)[5], rsrc_t wsr, int stok, int lane) {
#pragma unroll
    for (int i = 0; i < 5; ++i) sv[i] = ld16(wsr, WS_H4 + (stok >> 5) * HS + ((lane >> 1) + 32 * i) * 256 + (stok & 31) * 8 + (lane & 1) * 16);
}
DEVI u32 ln2_calc(const u4 (&sv)[5], int lane) {
    float v[4] = {0.f, 0.f, 0.f, 0.f};
#pragma unroll
    for (int i = 0; i < 5; ++i)
#pragma unroll
        for (int j = 0; j < 4; ++j) {
            const float a = bf_lo(sv[i][j]), b = bf_hi(sv[i][j]);
            v[j >> 1] = addS(addS(v[j >> 1], a), b); v[2 + (j >> 1)] = fmaS(b, b, fmaS(a, a, v[2 + (j >> 1)]));
        }
#pragma unroll
    for (int k = 0; k < 4; ++k) {
        v[k] = opq(v[k] + dppf<0x128>(v[k])); v[k] = opq(v[k] + dppf<0x124>(v[k])); v[k] = opq(v[k] + dppf<0x122>(v[k]));
        const auto r16 = __builtin_amdgcn_permlane16_swap(__float_as_uint(v[k]), __float_as_uint(v[k]), false, false);
        v[k] = addS(__uint_as_float(r16[0]), __uint_as_float(r16[1]));
        const auto r32 = __builtin_amdgcn_permlane32_swap(__float_as_uint(v[k]), __float_as_uint(v[k]), false, false);
        v[k] = addS(__uint_as_float(r32[0]), __uint_as_float(r32[1]));
    }
    return ln_pack((lane & 2) ? v[1] : v[0], (lane & 2) ? v[3] : v[2]);
}
DEVI void ln2_put(rsrc_t wsr, int stok, u32 pk, u32 tagv, int lane) {
    if (lane < 4) st8(wsr, WS_STAT2 + (stok + 2 * (lane & 1) + (lane >> 1)) * 8, u2{pk, tagv});
}

#define TOUCH_DELAY 12
#define TOUCH_DELAY2 8
#define LN2_DELAY 12
#define WPS 0
#define EPRIO 2
#define XA_DELAY 2
#define GATE 8
#define AHELP 4
#define P2T 0
#define ASH 13
#define NE0 0
#define APRE 6
#define W2_DELAY 8
#define MAP2 1
#define BPRE 8
#define P2PF 0
#define PACE() WAITV(8)
#define LPRE 6
#define LP0 19
#define LPRIO 0
#define TOUCHB 0
#define PACE0() WAITV(6)
#define TOUCHA 0
#define S2_DELAY 4
#define S1_DELAY 0
#define awaitq await
#define KPRIO 2
#define SPRIO 1
#define POLLB_FIRST 0
#define LN2E 0
#define XB_DELAY 40

__global__ __launch_bounds__(512, 2) void k_all(const u16* __restrict__ x, const u16* __restrict__ lar, const u16* __restrict__ aim,
                                                const u16* __restrict__ cre, const u16* __restrict__ cim, const u16* __restrict__ ldt,
                                                const u16* __restrict__ dsk, const u16* __restrict__ w1, const u16* __restrict__ w3,
                                                const u16* __restrict__ w2, char* __restrict__ ws, u16* __restrict__ out) {
    extern __shared__ __attribute__((aligned(16))) char smem[];
    STAMP(0);
    const int tid = threadIdx.x, lane = tid & 63;
    u32 pfc = 0;
    {
        unsigned long long pc; u32 len;
        asm volatile("s_getpc_b64 %0\n.Lpf_here%=:\n\ts_mov_b32 %1, .Lk_all_end - .Lpf_here%=" : "=s"(pc), "=s"(len));
        const u32 off = (u32)((511 - tid) * 32 + (blockIdx.x >> 3)) * 128u;
        if (off < len) pfc = *(const __attribute__((address_space(1))) u32*)(pc + off);
    }
    asm volatile("" :: "s"(x), "s"(lar), "s"(aim), "s"(cre), "s"(cim), "s"(ldt), "s"(dsk), "s"(w1), "s"(w3), "s"(w2), "s"(ws), "s"(out));
    const int w = __builtin_amdgcn_readfirstlane(tid >> 6);
    const int xcd = blockIdx.x & 7, jj = blockIdx.x >> 3;
    const rsrc_t wsr = __builtin_amdgcn_make_buffer_rsrc(ws, 0, WS_END, 0x00020000);
    u32 tagv, tagr;
#define TAG() do { asm volatile("" : "+v"(tagr)); tagv = __builtin_amdgcn_readfirstlane(tagr) + 1u; } while (0)
    u32 a_l, a_a, a_r, a_i, b_l, b_a, b_r, b_i, dtw;
    {
        int j2 = jj, w2_ = w;
        asm volatile("" : "+s"(j2), "+s"(w2_));
        if (j2 < 20 && w2_ < 4) {
            const int d = (xcd * 20 + jj) * 4 + w, po = d * NS;
            a_l = *(const u32*)(lar + po + 2 * lane); a_a = *(const u32*)(aim + po + 2 * lane);
            a_r = *(const u32*)(cre + po + 2 * lane); a_i = *(const u32*)(cim + po + 2 * lane);
            b_l = lar[po + 128 + lane]; b_a = aim[po + 128 + lane]; b_r = cre[po + 128 + lane]; b_i = cim[po + 128 + lane];
            dtw = ((const u32*)ldt)[d >> 1];
        }
        tagr = ld4(wsr, WS_EPOCH);
        FENCE();
        const int unit = (w2_ - 5) * 96 + (j2 - 20) * 8 + xcd;
        if (j2 >= 20 && w2_ >= 5 && unit < 256) {
            const u16* xp = x + (size_t)(unit * 8 + 2 * (lane >> 4)) * D + (lane & 15) * 8;
            u4 xv[2][5];
#pragma unroll
            for (int p = 0; p < 2; ++p)
#pragma unroll
                for (int i = 0; i < 5; ++i) xv[p][i] = *(const u4*)(xp + p * D + 128 * i);
            u32 pk[2];
#pragma unroll
            for (int p = 0; p < 2; ++p) {
                float s = 0.f, q = 0.f;
#pragma unroll
                for (int i = 0; i < 5; ++i)
#pragma unroll
                    for (int j = 0; j < 4; ++j) { s = dot2(xv[p][i][j], 0x3f803f80u, s); q = dot2(xv[p][i][j], xv[p][i][j], q); }
                pk[p] = ln_pack(row_sum16(s), row_sum16(q));
            }
            if ((lane & 15) < 8) st16(wsr, WS_REP + (lane & 7) * REPS + RP_STATS + unit * 64 + (lane >> 4) * 16, u4{pk[0], tagr + 1u, pk[1], tagr + 1u});
            STAMP(5);
        }
        FENCE();
    }
    const int mb = (xcd & 4) + (jj & 3), n1 = (xcd & 3) * 8 + (jj >> 2);
#define UB0(n) (((n) >> 3) * 24 + (((n) & 7) < 4 ? 4 * ((n) & 7) : ((n) & 7) == 4 ? 21 : ((n) & 7) == 7 ? 3 : 3 * ((n) & 7)))
#define UBS(n) (((n) & 7) == 7 ? 4 : 1)
    const int ub0 = UB0(n1), ubs = UBS(n1);
    const u32 lds0 = (u32)(size_t)(LAS char*)smem;
    if (tid < 64) ((u32*)(smem + LDS_CNT))[tid] = 0u;
    const int rep = WS_REP + xcd * REPS;
    const int mt = (xcd >> 1) * 8 + (jj & 7), nt = (xcd & 1) * 4 + (jj >> 3);

    if (jj >= 20) {
        const int nb = (jj - 20) * 8 + xcd;
        const int tile = 1024 + (w & 3) * 8448 + (w == 7 ? -8448 : 0);
        if (w < 3) __builtin_amdgcn_s_sleep(TOUCH_DELAY); else BAR();
        TAG();
        if (w < 3) {
            if (nb < 80) {
                const u16* src = (nb < 40 ? w1 : w3) + (size_t)(nb % 40) * 16 * F + w * 512 + lane * 8;
                u4 cv[16];
#pragma unroll
                for (int i = 0; i < 16; ++i) cv[i] = *(const u4*)(src + i * F);
                SB();
                BAR();
                f2 a[4] = {};
#pragma unroll
                for (int i = 0; i < 16; ++i)
#pragma unroll
                    for (int j = 0; j < 4; ++j) a[j] += un_bf(cv[i][j]);
                sig(smem, cK, lane);
                STAMP(1);
                const int o = WS_CS + (nb * F + w * 512 + lane * 8) * 4;
                st16(wsr, o, __builtin_bit_cast(u4, f4{a[0].x, a[0].y, a[1].x, a[1].y})); st16(wsr, o + 16, __builtin_bit_cast(u4, f4{a[2].x, a[2].y, a[3].x, a[3].y}));
            } else { BAR(); __builtin_amdgcn_s_sleep(W2_DELAY); sig(smem, cK, lane); }
        } else if (w < 5 || (w == 7 && nb >= 48)) {
            await(smem, cK, 3);
            const int u = w < 5 ? nb * 2 + (w - 3) : 144 + nb, rb = u / 5, cb = u % 5;
            const char* src = uni((const char*)w2 + (size_t)rb * 32 * (D * 2) + cb * 256);
#pragma unroll
            for (int i = 0; i < 8; ++i) glds16(src + i * 4 * (D * 2), (lane >> 4) * (D * 2) + (lane & 15) * 16, lds0 + tile + i * 1056);
            WAITV(0);
            STAMP(1);
            const char* q = smem + tile + (lane >> 4) * 2112 + ((lane & 15) >> 2) * 256 + (lane & 3) * 8;
            b8 fr[8];
#pragma unroll
            for (int i = 0; i < 8; ++i) fr[i] = cat(trd(q + i * 32), trd(q + i * 32 + 1056));
#pragma unroll
            for (int i = 0; i < 8; ++i) {
                const int ctg = cb * 8 + i;
                st16(wsr, WS_P2 + (ctg / 5) * P2S + (rb * 5 + ctg % 5) * 1024 + lane * 16, __builtin_bit_cast(u4, fr[i]));
            }
        }
        STAMP(2);
        WAITV(0);
        if (sig_ret(smem, cY2, lane) == 7u && lane < 8) st4(wsr, WS_REP + lane * REPS + RP_PACK + nb * FS, tagv);
        await(smem, cY2, 8);
        STAMP(3);
        stage_w13(w1, w3, ub0, ubs, 39 - w, -1, -8, lds0, lane);
        WAITV(0);
        sig(smem, cW, lane);
        STAMP(4);
        if (w == 6) {
    pollN(wsr, rep + RP_PACK, FS, 96, -1, tagv, lane);
    sig(smem, cPk, lane);
}
        if (TOUCHA && w == 1) touch_h(wsr, rep + RP_FLAG1, WS_H4 + (xcd & 4) * 4 * HS, jj, tagv, lane);
    } else if (w < 4) {
        const int d = (xcd * 20 + jj) * 4 + w;
        BAR();
        asm volatile("" : "+v"(a_l), "+v"(a_a), "+v"(a_r), "+v"(a_i), "+v"(b_l), "+v"(b_a), "+v"(b_r), "+v"(b_i), "+v"(dtw));
        const float dt = rbf(exp_acc((d & 1) ? bf_hi(dtw) : bf_lo(dtw)));
        float cmax = fmaxf(fmaxf(fmaxf(fabsf(bf_lo(a_r)), fabsf(bf_hi(a_r))), fmaxf(fabsf(bf_lo(a_i)), fabsf(bf_hi(a_i)))), fmaxf(fabsf(bf_lo(b_r)), fabsf(bf_lo(b_i))));
        cmax = fmaxf(cmax, dppf<0x128>(cmax)); cmax = fmaxf(cmax, dppf<0x124>(cmax)); cmax = fmaxf(cmax, dppf<0x122>(cmax)); cmax = fmaxf(cmax, dppf<0x121>(cmax));
        cmax = fmaxf(fmaxf(rl(cmax, 0), rl(cmax, 16)), fmaxf(rl(cmax, 32), rl(cmax, 48)));
        const int ce = __builtin_amdgcn_frexp_expf(cmax);
        const float cs = __builtin_amdgcn_ldexpf(1.f, min(7 - ce, 126));
        const float yscale = 2.f * dt * __builtin_amdgcn_ldexpf(1.f, -min(7 - ce, 126));
        asm volatile("" :: "v"(cs), "v"(a_l), "v"(b_l), "v"(a_a), "v"(b_a), "v"(dt));
        STAMP(1);
        u32 wo[3];
#pragma unroll
        for (int i = 0; i < 3; ++i) { const int pc = i * 64 + lane; wo[i] = (pc / 6) * (F * 2) + (pc % 6) * 16; asm volatile("" : "+v"(wo[i])); }
        const char* wsrc = uni((const char*)w1 + (size_t)w * 32 * (F * 2) + ub0 * 32);
        f16v kacc;
        {
            f2 zz[3][4];
#pragma unroll
            for (int i = 0; i < 3; ++i) {
                const float vl = i == 0 ? bf_lo(a_l) : i == 1 ? bf_hi(a_l) : bf_lo(b_l);
                const float va = i == 0 ? bf_lo(a_a) : i == 1 ? bf_hi(a_a) : bf_lo(b_a);
                const float vr = i == 0 ? bf_lo(a_r) : i == 1 ? bf_hi(a_r) : bf_lo(b_r);
                const float vi = i == 0 ? bf_lo(a_i) : i == 1 ? bf_hi(a_i) : bf_lo(b_i);
                const f2 e1 = f2{rbf(exp_acc(vl)), va} * dt * f2{-1.44269504088896341f, 0.15915494309189535f};
                const f2 e8 = e1 * 8.f, eb = e1 * 256.f;
                zz[i][0] = cexp_rev(e1.x, e1.y); zz[i][1] = cexp_rev(e8.x, e8.y); zz[i][2] = cexp_rev(eb.x, eb.y);
                zz[i][3] = f2{vr, vi} * cs;
            }
            const u32 fa = lds0 + w * CV_R + (lane & 31) * 272 + (lane >> 5) * 16, fy = lds0 + CV_Y0 + w * CV_Y + (lane & 31) * 272 + (lane >> 5) * 16;
            const u32 mx = lds0 + w * CV_R, my = lds0 + w * CV_Y;
            asm volatile("" :: "v"(zz[0][0]), "v"(zz[1][1]), "v"(zz[2][2]), "v"(zz[2][0]), "v"(zz[2][1]), "v"(zz[0][2]), "v"(zz[1][2]));
            STAMP(7);
    asm volatile(
        "s_mov_b32 s40, 0\n"
        "s_mov_b32 m0, %16\n"
        "v_mov_b32 v128, 0x3c00\n"
        "ds_write_addtid_b32 v128 offset:51456\n"
        ".p2align 3\n"
        ".Lkg%=:\n"
        "v_mov_b64 v[136:137], %2\n"
        "v_mov_b64 v[138:139], %4\n"
        "v_pk_mul_f32 v[122:123], %2, %2 op_sel:[1,1] op_sel_hi:[1,0] neg_lo:[0,1]\n"
        "v_pk_mul_f32 v[124:125], %4, %1 op_sel:[1,1] op_sel_hi:[1,0] neg_lo:[0,1]\n"
        "v_pk_fma_f32 v[120:121], %2, %2, v[122:123] op_sel_hi:[1,0,1]\n"
        "v_pk_fma_f32 v[140:141], %1, %4, v[124:125] op_sel_hi:[1,0,1]\n"
        "v_pk_mul_f32 v[122:123], v[136:137], v[120:121] op_sel:[1,1] op_sel_hi:[1,0] neg_lo:[0,1]\n"
        "v_pk_mul_f32 v[124:125], v[120:121], v[120:121] op_sel:[1,1] op_sel_hi:[1,0] neg_lo:[0,1]\n"
        "v_pk_mul_f32 v[126:127], v[140:141], %1 op_sel:[1,1] op_sel_hi:[1,0] neg_lo:[0,1]\n"
        "v_pk_fma_f32 v[142:143], v[120:121], v[136:137], v[122:123] op_sel_hi:[1,0,1]\n"
        "v_pk_fma_f32 v[144:145], v[120:121], v[120:121], v[124:125] op_sel_hi:[1,0,1]\n"
        "v_pk_fma_f32 v[146:147], %1, v[140:141], v[126:127] op_sel_hi:[1,0,1]\n"
        "v_pk_mul_f32 v[122:123], v[142:143], v[120:121] op_sel:[1,1] op_sel_hi:[1,0] neg_lo:[0,1]\n"
        "v_pk_mul_f32 v[124:125], v[144:145], v[120:121] op_sel:[1,1] op_sel_hi:[1,0] neg_lo:[0,1]\n"
        "v_pk_mul_f32 v[126:127], v[146:147], %1 op_sel:[1,1] op_sel_hi:[1,0] neg_lo:[0,1]\n"
        "v_pk_fma_f32 v[148:149], v[120:121], v[142:143], v[122:123] op_sel_hi:[1,0,1]\n"
        "v_pk_fma_f32 v[150:151], v[120:121], v[144:145], v[124:125] op_sel_hi:[1,0,1]\n"
        "v_pk_fma_f32 v[152:153], %1, v[146:147], v[126:127] op_sel_hi:[1,0,1]\n"
        "v_pk_mul_f32 v[122:123], v[148:149], v[120:121] op_sel:[1,1] op_sel_hi:[1,0] neg_lo:[0,1]\n"
        "v_pk_mul_f32 v[124:125], v[150:151], v[120:121] op_sel:[1,1] op_sel_hi:[1,0] neg_lo:[0,1]\n"
        "v_pk_mul_f32 v[126:127], v[152:153], %1 op_sel:[1,1] op_sel_hi:[1,0] neg_lo:[0,1]\n"
        "v_pk_fma_f32 v[154:155], v[120:121], v[148:149], v[122:123] op_sel_hi:[1,0,1]\n"
        "v_pk_fma_f32 v[156:157], v[120:121], v[150:151], v[124:125] op_sel_hi:[1,0,1]\n"
        "v_pk_fma_f32 v[158:159], %1, v[152:153], v[126:127] op_sel_hi:[1,0,1]\n"
        "v_pk_mul_f32 v[122:123], v[154:155], v[120:121] op_sel:[1,1] op_sel_hi:[1,0] neg_lo:[0,1]\n"
        "v_pk_mul_f32 v[124:125], v[156:157], v[120:121] op_sel:[1,1] op_sel_hi:[1,0] neg_lo:[0,1]\n"
        "v_pk_mul_f32 v[126:127], v[158:159], %1 op_sel:[1,1] op_sel_hi:[1,0] neg_lo:[0,1]\n"
        "v_pk_fma_f32 v[160:161], v[120:121], v[154:155], v[122:123] op_sel_hi:[1,0,1]\n"
        "v_pk_fma_f32 v[162:163], v[120:121], v[156:157], v[124:125] op_sel_hi:[1,0,1]\n"
        "v_pk_fma_f32 v[164:165], %1, v[158:159], v[126:127] op_sel_hi:[1,0,1]\n"
        "v_pk_mul_f32 v[122:123], v[160:161], v[120:121] op_sel:[1,1] op_sel_hi:[1,0] neg_lo:[0,1]\n"
        "v_pk_mul_f32 v[124:125], v[162:163], v[120:121] op_sel:[1,1] op_sel_hi:[1,0] neg_lo:[0,1]\n"
        "v_pk_mul_f32 v[126:127], v[164:165], %1 op_sel:[1,1] op_sel_hi:[1,0] neg_lo:[0,1]\n"
        "v_pk_fma_f32 v[166:167], v[120:121], v[160:161], v[122:123] op_sel_hi:[1,0,1]\n"
        "v_pk_fma_f32 v[168:169], v[120:121], v[162:163], v[124:125] op_sel_hi:[1,0,1]\n"
        "v_pk_fma_f32 v[170:171], %1, v[164:165], v[126:127] op_sel_hi:[1,0,1]\n"
        "v_pk_mul_f32 v[122:123], v[166:167], v[120:121] op_sel:[1,1] op_sel_hi:[1,0] neg_lo:[0,1]\n"
        "v_pk_mul_f32 v[124:125], v[168:169], v[120:121] op_sel:[1,1] op_sel_hi:[1,0] neg_lo:[0,1]\n"
        "v_pk_mul_f32 v[126:127], v[170:171], %1 op_sel:[1,1] op_sel_hi:[1,0] neg_lo:[0,1]\n"
        "v_pk_fma_f32 v[172:173], v[120:121], v[166:167], v[122:123] op_sel_hi:[1,0,1]\n"
        "v_pk_fma_f32 v[174:175], v[120:121], v[168:169], v[124:125] op_sel_hi:[1,0,1]\n"
        "v_pk_fma_f32 v[176:177], %1, v[170:171], v[126:127] op_sel_hi:[1,0,1]\n"
        "v_pk_mul_f32 v[122:123], v[172:173], v[120:121] op_sel:[1,1] op_sel_hi:[1,0] neg_lo:[0,1]\n"
        "v_pk_mul_f32 v[124:125], v[176:177], %1 op_sel:[1,1] op_sel_hi:[1,0] neg_lo:[0,1]\n"
        "v_pk_fma_f32 v[178:179], v[120:121], v[172:173], v[122:123] op_sel_hi:[1,0,1]\n"
        "v_pk_fma_f32 v[180:181], %1, v[176:177], v[124:125] op_sel_hi:[1,0,1]\n"
        "s_waitcnt lgkmcnt(0)\n"
        "s_nop 0\n"
        "v_cvt_pk_f16_f32 v128, v136, v137\n"
        "v_mfma_f32_32x32x16_f16 %0, v[192:195], v[224:227], %0\n"
        "v_cvt_pk_f16_f32 v129, v120, v121\n"
        "v_cvt_pk_f16_f32 v130, v142, v143\n"
        "v_cvt_pk_f16_f32 v131, v144, v145\n"
        "v_cvt_pk_f16_f32 v132, v148, v149\n"
        "v_mfma_f32_32x32x16_f16 %0, v[196:199], v[228:231], %0\n"
        "v_cvt_pk_f16_f32 v133, v150, v151\n"
        "v_cvt_pk_f16_f32 v134, v154, v155\n"
        "v_cvt_pk_f16_f32 v135, v156, v157\n"
        "s_mov_b32 m0, %16\n"
        "s_nop 0\n"
        "ds_write_addtid_b32 v128 offset:51728\n"
        "ds_write_addtid_b32 v129 offset:52000\n"
        "ds_write_addtid_b32 v130 offset:52272\n"
        "ds_write_addtid_b32 v131 offset:52544\n"
        "v_mfma_f32_32x32x16_f16 %0, v[200:203], v[232:235], %0\n"
        "ds_write_addtid_b32 v132 offset:52816\n"
        "ds_write_addtid_b32 v133 offset:53088\n"
        "ds_write_addtid_b32 v134 offset:53360\n"
        "ds_write_addtid_b32 v135 offset:53632\n"
        "v_cvt_pk_f16_f32 v128, v160, v161\n"
        "v_mfma_f32_32x32x16_f16 %0, v[204:207], v[236:239], %0\n"
        "v_cvt_pk_f16_f32 v129, v162, v163\n"
        "v_cvt_pk_f16_f32 v130, v166, v167\n"
        "v_cvt_pk_f16_f32 v131, v168, v169\n"
        "v_cvt_pk_f16_f32 v132, v172, v173\n"
        "v_mfma_f32_32x32x16_f16 %0, v[208:211], v[240:243], %0\n"
        "v_cvt_pk_f16_f32 v133, v174, v175\n"
        "v_cvt_pk_f16_f32 v134, v178, v179\n"
        "s_mov_b32 m0, %16\n"
        "s_nop 0\n"
        "ds_write_addtid_b32 v128 offset:53904\n"
        "ds_write_addtid_b32 v129 offset:54176\n"
        "ds_write_addtid_b32 v130 offset:54448\n"
        "ds_write_addtid_b32 v131 offset:54720\n"
        "v_mfma_f32_32x32x16_f16 %0, v[212:215], v[244:247], %0\n"
        "ds_write_addtid_b32 v132 offset:54992\n"
        "ds_write_addtid_b32 v133 offset:55264\n"
        "ds_write_addtid_b32 v134 offset:55536\n"
        "v_cvt_pk_f16_f32 v128, v138, -v139\n"
        "v_mfma_f32_32x32x16_f16 %0, v[216:219], v[248:251], %0\n"
        "v_cvt_pk_f16_f32 v129, v140, -v141\n"
        "v_cvt_pk_f16_f32 v130, v146, -v147\n"
        "v_cvt_pk_f16_f32 v131, v152, -v153\n"
        "v_cvt_pk_f16_f32 v132, v158, -v159\n"
        "v_mfma_f32_32x32x16_f16 %0, v[220:223], v[252:255], %0\n"
        "v_cvt_pk_f16_f32 v133, v164, -v165\n"
        "v_cvt_pk_f16_f32 v134, v170, -v171\n"
        "v_cvt_pk_f16_f32 v135, v176, -v177\n"
        "s_mov_b32 m0, %15\n"
        "s_nop 0\n"
        "ds_write_addtid_b32 v128 offset:0\n"
        "ds_write_addtid_b32 v129 offset:272\n"
        "ds_write_addtid_b32 v130 offset:544\n"
        "ds_write_addtid_b32 v131 offset:816\n"
        "ds_write_addtid_b32 v132 offset:1088\n"
        "ds_write_addtid_b32 v132 offset:6800\n"
        "ds_write_addtid_b32 v133 offset:1360\n"
        "ds_write_addtid_b32 v134 offset:1632\n"
        "ds_write_addtid_b32 v135 offset:1904\n"
        "v_pk_mul_f32 v[122:123], v[174:175], v[120:121] op_sel:[1,1] op_sel_hi:[1,0] neg_lo:[0,1]\n"
        "v_pk_mul_f32 v[124:125], v[178:179], v[120:121] op_sel:[1,1] op_sel_hi:[1,0] neg_lo:[0,1]\n"
        "v_pk_mul_f32 v[126:127], v[138:139], %3 op_sel:[1,1] op_sel_hi:[1,0] neg_lo:[0,1]\n"
        "v_pk_fma_f32 v[192:193], v[120:121], v[174:175], v[122:123] op_sel_hi:[1,0,1]\n"
        "v_pk_fma_f32 v[194:195], v[120:121], v[178:179], v[124:125] op_sel_hi:[1,0,1]\n"
        "v_pk_fma_f32 v[196:197], %3, v[138:139], v[126:127] op_sel_hi:[1,0,1]\n"
        "v_pk_mul_f32 v[122:123], v[192:193], v[120:121] op_sel:[1,1] op_sel_hi:[1,0] neg_lo:[0,1]\n"
        "v_pk_mul_f32 v[124:125], v[194:195], v[120:121] op_sel:[1,1] op_sel_hi:[1,0] neg_lo:[0,1]\n"
        "v_pk_mul_f32 v[126:127], v[196:197], %3 op_sel:[1,1] op_sel_hi:[1,0] neg_lo:[0,1]\n"
        "v_pk_fma_f32 v[198:199], v[120:121], v[192:193], v[122:123] op_sel_hi:[1,0,1]\n"
        "v_pk_fma_f32 v[200:201], v[120:121], v[194:195], v[124:125] op_sel_hi:[1,0,1]\n"
        "v_pk_fma_f32 v[202:203], %3, v[196:197], v[126:127] op_sel_hi:[1,0,1]\n"
        "v_pk_mul_f32 v[122:123], v[198:199], v[120:121] op_sel:[1,1] op_sel_hi:[1,0] neg_lo:[0,1]\n"
        "v_pk_mul_f32 v[124:125], v[200:201], v[120:121] op_sel:[1,1] op_sel_hi:[1,0] neg_lo:[0,1]\n"
        "v_pk_mul_f32 v[126:127], v[202:203], %3 op_sel:[1,1] op_sel_hi:[1,0] neg_lo:[0,1]\n"
        "v_pk_fma_f32 v[204:205], v[120:121], v[198:199], v[122:123] op_sel_hi:[1,0,1]\n"
        "v_pk_fma_f32 v[206:207], v[120:121], v[200:201], v[124:125] op_sel_hi:[1,0,1]\n"
        "v_pk_fma_f32 v[208:209], %3, v[202:203], v[126:127] op_sel_hi:[1,0,1]\n"
        "v_pk_mul_f32 v[122:123], v[204:205], v[120:121] op_sel:[1,1] op_sel_hi:[1,0] neg_lo:[0,1]\n"
        "v_pk_mul_f32 v[124:125], v[206:207], v[120:121] op_sel:[1,1] op_sel_hi:[1,0] neg_lo:[0,1]\n"
        "v_pk_mul_f32 v[126:127], v[208:209], %3 op_sel:[1,1] op_sel_hi:[1,0] neg_lo:[0,1]\n"
        "v_pk_fma_f32 v[210:211], v[120:121], v[204:205], v[122:123] op_sel_hi:[1,0,1]\n"
        "v_pk_fma_f32 v[212:213], v[120:121], v[206:207], v[124:125] op_sel_hi:[1,0,1]\n"
        "v_pk_fma_f32 v[214:215], %3, v[208:209], v[126:127] op_sel_hi:[1,0,1]\n"
        "v_pk_mul_f32 v[122:123], v[210:211], v[120:121] op_sel:[1,1] op_sel_hi:[1,0] neg_lo:[0,1]\n"
        "v_pk_mul_f32 v[124:125], v[212:213], v[120:121] op_sel:[1,1] op_sel_hi:[1,0] neg_lo:[0,1]\n"
        "v_pk_mul_f32 v[126:127], v[214:215], %3 op_sel:[1,1] op_sel_hi:[1,0] neg_lo:[0,1]\n"
        "v_pk_fma_f32 v[216:217], v[120:121], v[210:211], v[122:123] op_sel_hi:[1,0,1]\n"
        "v_pk_fma_f32 v[218:219], v[120:121], v[212:213], v[124:125] op_sel_hi:[1,0,1]\n"
        "v_pk_fma_f32 v[220:221], %3, v[214:215], v[126:127] op_sel_hi:[1,0,1]\n"
        "v_pk_mul_f32 v[122:123], v[216:217], v[120:121] op_sel:[1,1] op_sel_hi:[1,0] neg_lo:[0,1]\n"
        "v_pk_mul_f32 v[124:125], v[218:219], v[120:121] op_sel:[1,1] op_sel_hi:[1,0] neg_lo:[0,1]\n"
        "v_pk_mul_f32 v[126:127], v[220:221], %3 op_sel:[1,1] op_sel_hi:[1,0] neg_lo:[0,1]\n"
        "v_pk_fma_f32 v[222:223], v[120:121], v[216:217], v[122:123] op_sel_hi:[1,0,1]\n"
        "v_pk_fma_f32 v[224:225], v[120:121], v[218:219], v[124:125] op_sel_hi:[1,0,1]\n"
        "v_pk_fma_f32 v[226:227], %3, v[220:221], v[126:127] op_sel_hi:[1,0,1]\n"
        "v_pk_mul_f32 v[122:123], v[222:223], v[120:121] op_sel:[1,1] op_sel_hi:[1,0] neg_lo:[0,1]\n"
        "v_pk_mul_f32 v[124:125], v[224:225], v[120:121] op_sel:[1,1] op_sel_hi:[1,0] neg_lo:[0,1]\n"
        "v_pk_mul_f32 v[126:127], v[226:227], %3 op_sel:[1,1] op_sel_hi:[1,0] neg_lo:[0,1]\n"
        "v_pk_fma_f32 v[228:229], v[120:121], v[222:223], v[122:123] op_sel_hi:[1,0,1]\n"
        "v_pk_fma_f32 v[230:231], v[120:121], v[224:225], v[124:125] op_sel_hi:[1,0,1]\n"
        "v_pk_fma_f32 v[232:233], %3, v[226:227], v[126:127] op_sel_hi:[1,0,1]\n"
        "v_pk_mul_f32 v[122:123], v[228:229], v[120:121] op_sel:[1,1] op_sel_hi:[1,0] neg_lo:[0,1]\n"
        "v_pk_mul_f32 v[124:125], v[230:231], v[120:121] op_sel:[1,1] op_sel_hi:[1,0] neg_lo:[0,1]\n"
        "v_pk_mul_f32 v[126:127], v[232:233], %3 op_sel:[1,1] op_sel_hi:[1,0] neg_lo:[0,1]\n"
        "v_pk_fma_f32 v[234:235], v[120:121], v[228:229], v[122:123] op_sel_hi:[1,0,1]\n"
        "v_pk_fma_f32 v[236:237], v[120:121], v[230:231], v[124:125] op_sel_hi:[1,0,1]\n"
        "v_pk_fma_f32 v[238:239], %3, v[232:233], v[126:127] op_sel_hi:[1,0,1]\n"
        "v_pk_mul_f32 v[122:123], v[158:159], %3 op_sel:[1,1] op_sel_hi:[1,0] neg_lo:[0,1]\n"
        "v_pk_mul_f32 v[124:125], v[146:147], %3 op_sel:[1,1] op_sel_hi:[1,0] neg_lo:[0,1]\n"
        "v_pk_fma_f32 v[240:241], %3, v[158:159], v[122:123] op_sel_hi:[1,0,1]\n"
        "v_pk_fma_f32 v[242:243], %3, v[146:147], v[124:125] op_sel_hi:[1,0,1]\n"
        "v_pk_mul_f32 v[122:123], v[240:241], %3 op_sel:[1,1] op_sel_hi:[1,0] neg_lo:[0,1]\n"
        "v_pk_mul_f32 v[124:125], v[170:171], %3 op_sel:[1,1] op_sel_hi:[1,0] neg_lo:[0,1]\n"
        "v_pk_fma_f32 v[244:245], %3, v[240:241], v[122:123] op_sel_hi:[1,0,1]\n"
        "v_pk_fma_f32 v[246:247], %3, v[170:171], v[124:125] op_sel_hi:[1,0,1]\n"
        "v_pk_mul_f32 v[122:123], v[244:245], %3 op_sel:[1,1] op_sel_hi:[1,0] neg_lo:[0,1]\n"
        "s_nop 0\n"
        "s_nop 0\n"
        "v_pk_fma_f32 v[248:249], %3, v[244:245], v[122:123] op_sel_hi:[1,0,1]\n"
        "s_nop 0\n"
        "s_nop 0\n"
        "v_cvt_pk_f16_f32 v128, v192, v193\n"
        "v_cvt_pk_f16_f32 v129, v194, v195\n"
        "v_cvt_pk_f16_f32 v130, v198, v199\n"
        "v_cvt_pk_f16_f32 v131, v200, v201\n"
        "v_cvt_pk_f16_f32 v132, v204, v205\n"
        "v_cvt_pk_f16_f32 v133, v206, v207\n"
        "v_cvt_pk_f16_f32 v134, v210, v211\n"
        "v_cvt_pk_f16_f32 v135, v212, v213\n"
        "s_mov_b32 m0, %16\n"
        "s_nop 0\n"
        "ds_write_addtid_b32 v128 offset:55808\n"
        "ds_write_addtid_b32 v129 offset:56080\n"
        "ds_write_addtid_b32 v130 offset:56352\n"
        "ds_write_addtid_b32 v131 offset:56624\n"
        "ds_write_addtid_b32 v132 offset:56896\n"
        "ds_write_addtid_b32 v133 offset:57168\n"
        "ds_write_addtid_b32 v134 offset:57440\n"
        "ds_write_addtid_b32 v135 offset:57712\n"
        "v_cvt_pk_f16_f32 v128, v216, v217\n"
        "v_cvt_pk_f16_f32 v129, v218, v219\n"
        "v_cvt_pk_f16_f32 v130, v222, v223\n"
        "v_cvt_pk_f16_f32 v131, v224, v225\n"
        "v_cvt_pk_f16_f32 v132, v228, v229\n"
        "v_cvt_pk_f16_f32 v133, v230, v231\n"
        "v_cvt_pk_f16_f32 v134, v234, v235\n"
        "v_cvt_pk_f16_f32 v135, v236, v237\n"
        "s_mov_b32 m0, %16\n"
        "s_nop 0\n"
        "ds_write_addtid_b32 v128 offset:57984\n"
        "ds_write_addtid_b32 v129 offset:58256\n"
        "ds_write_addtid_b32 v130 offset:58528\n"
        "ds_write_addtid_b32 v131 offset:58800\n"
        "ds_write_addtid_b32 v132 offset:59072\n"
        "ds_write_addtid_b32 v133 offset:59344\n"
        "ds_write_addtid_b32 v134 offset:59616\n"
        "ds_write_addtid_b32 v135 offset:59888\n"
        "v_cvt_pk_f16_f32 v128, v180, -v181\n"
        "v_cvt_pk_f16_f32 v129, v196, -v197\n"
        "v_cvt_pk_f16_f32 v130, v242, -v243\n"
        "v_cvt_pk_f16_f32 v131, v240, -v241\n"
        "v_cvt_pk_f16_f32 v132, v246, -v247\n"
        "v_cvt_pk_f16_f32 v133, v202, -v203\n"
        "v_cvt_pk_f16_f32 v134, v244, -v245\n"
        "v_cvt_pk_f16_f32 v135, v208, -v209\n"
        "s_mov_b32 m0, %15\n"
        "s_nop 0\n"
        "ds_write_addtid_b32 v128 offset:7888\n"
        "ds_write_addtid_b32 v129 offset:2176\n"
        "ds_write_addtid_b32 v130 offset:2448\n"
        "ds_write_addtid_b32 v131 offset:2720\n"
        "ds_write_addtid_b32 v132 offset:2992\n"
        "ds_write_addtid_b32 v133 offset:3264\n"
        "ds_write_addtid_b32 v133 offset:4352\n"
        "ds_write_addtid_b32 v134 offset:4624\n"
        "ds_write_addtid_b32 v135 offset:5440\n"
        "v_cvt_pk_f16_f32 v128, v248, -v249\n"
        "v_cvt_pk_f16_f32 v129, v214, -v215\n"
        "v_cvt_pk_f16_f32 v130, v220, -v221\n"
        "v_cvt_pk_f16_f32 v131, v226, -v227\n"
        "v_cvt_pk_f16_f32 v132, v232, -v233\n"
        "v_cvt_pk_f16_f32 v133, v238, -v239\n"
        "s_mov_b32 m0, %15\n"
        "s_nop 0\n"
        "ds_write_addtid_b32 v128 offset:5712\n"
        "ds_write_addtid_b32 v129 offset:4896\n"
        "ds_write_addtid_b32 v130 offset:5984\n"
        "ds_write_addtid_b32 v131 offset:5168\n"
        "ds_write_addtid_b32 v132 offset:6256\n"
        "ds_write_addtid_b32 v133 offset:6528\n"
        "ds_read_b128 v[192:195], %13 offset:0\n"
        "ds_read_b128 v[224:227], %14 offset:0\n"
        "ds_read_b128 v[196:199], %13 offset:32\n"
        "ds_read_b128 v[228:231], %14 offset:32\n"
        "ds_read_b128 v[200:203], %13 offset:64\n"
        "ds_read_b128 v[232:235], %14 offset:64\n"
        "ds_read_b128 v[204:207], %13 offset:96\n"
        "ds_read_b128 v[236:239], %14 offset:96\n"
        "ds_read_b128 v[208:211], %13 offset:128\n"
        "ds_read_b128 v[240:243], %14 offset:128\n"
        "ds_read_b128 v[212:215], %13 offset:160\n"
        "ds_read_b128 v[244:247], %14 offset:160\n"
        "ds_read_b128 v[216:219], %13 offset:192\n"
        "ds_read_b128 v[248:251], %14 offset:192\n"
        "ds_read_b128 v[220:223], %13 offset:224\n"
        "ds_read_b128 v[252:255], %14 offset:224\n"
        "v_mov_b64 %1, %5\n"
        "v_mov_b64 %2, %6\n"
        "v_mov_b64 %3, %7\n"
        "v_mov_b64 %4, %8\n"
        "v_mov_b64 %5, %9\n"
        "v_mov_b64 %6, %10\n"
        "v_mov_b64 %7, %11\n"
        "v_mov_b64 %8, %12\n"
        "s_mov_b64 exec, 1\n"
        "ds_add_u32 %17, %18\n"
        "s_mov_b64 exec, -1\n"
        "s_cmp_lg_u32 s40, 0\n"
        "s_cbranch_scc1 .Lkz%=\n"
        "v_mov_b32 v128, 0\n"
        "v_mov_b32 v129, 0\n"
        "v_mov_b32 v130, 0\n"
        "v_mov_b32 v131, 0\n"
        "s_nop 1\n"
        "v_mfma_f32_32x32x16_f16 %0, v[128:131], v[128:131], 0\n"
        ".Lkz%=:\n"
        "s_add_u32 s40, s40, 1\n"
        "s_cmp_lt_u32 s40, 3\n"
        "s_cbranch_scc1 .Lkg%=\n"
        "s_waitcnt lgkmcnt(15)\n"
        "v_mfma_f32_32x32x16_f16 %0, v[192:195], v[224:227], %0\n"
        "s_waitcnt lgkmcnt(13)\n"
        "v_mfma_f32_32x32x16_f16 %0, v[196:199], v[228:231], %0\n"
        "s_waitcnt lgkmcnt(11)\n"
        "v_mfma_f32_32x32x16_f16 %0, v[200:203], v[232:235], %0\n"
        "s_waitcnt lgkmcnt(9)\n"
        "v_mfma_f32_32x32x16_f16 %0, v[204:207], v[236:239], %0\n"
        "s_waitcnt lgkmcnt(7)\n"
        "v_mfma_f32_32x32x16_f16 %0, v[208:211], v[240:243], %0\n"
        "s_waitcnt lgkmcnt(5)\n"
        "v_mfma_f32_32x32x16_f16 %0, v[212:215], v[244:247], %0\n"
        "s_waitcnt lgkmcnt(3)\n"
        "v_mfma_f32_32x32x16_f16 %0, v[216:219], v[248:251], %0\n"
        "s_waitcnt lgkmcnt(1)\n"
        "v_mfma_f32_32x32x16_f16 %0, v[220:223], v[252:255], %0\n"
        "s_nop 7\n"
        "s_nop 3\n"
        : "=&v"(kacc), "+v"(zz[0][0]), "+v"(zz[0][1]), "+v"(zz[0][2]), "+v"(zz[0][3]), "+v"(zz[1][0]), "+v"(zz[1][1]), "+v"(zz[1][2]), "+v"(zz[1][3]), "+v"(zz[2][0]), "+v"(zz[2][1]), "+v"(zz[2][2]), "+v"(zz[2][3])
        : "v"(fa), "v"(fy), "s"(mx), "s"(my), "v"(cnt_addr(smem, cR2)), "v"(1u)
        : "memory", "s40", "scc", "v120", "v121", "v122", "v123", "v124", "v125", "v126", "v127", "v128", "v129", "v130", "v131", "v132", "v133", "v134", "v135", "v136", "v137", "v138", "v139", "v140", "v141", "v142", "v143", "v144", "v145", "v146", "v147", "v148", "v149", "v150", "v151", "v152", "v153", "v154", "v155", "v156", "v157", "v158", "v159", "v160", "v161", "v162", "v163", "v164", "v165", "v166", "v167", "v168", "v169", "v170", "v171", "v172", "v173", "v174", "v175", "v176", "v177", "v178", "v179", "v180", "v181", "v182", "v183", "v184", "v185", "v186", "v187", "v188", "v189", "v190", "v191", "v192", "v193", "v194", "v195", "v196", "v197", "v198", "v199", "v200", "v201", "v202", "v203", "v204", "v205", "v206", "v207", "v208", "v209", "v210", "v211", "v212", "v213", "v214", "v215", "v216", "v217", "v218", "v219", "v220", "v221", "v222", "v223", "v224", "v225", "v226", "v227", "v228", "v229", "v230", "v231", "v232", "v233", "v234", "v235", "v236", "v237", "v238", "v239", "v240", "v241", "v242", "v243", "v244", "v245", "v246", "v247", "v248", "v249", "v250", "v251", "v252", "v253", "v254", "v255");

        }
        sig(smem, cK, lane);
        TAG();
        STAMP(2);
        char* rt = smem + w * CV_R + 64;
        if (lane < 4) *(u4*)(rt - 64 + lane * 16) = u4{0, 0, 0, (lane == 3) ? pk_h(rl(kacc[0], 0), 0.f) : 0u};
        *(u4*)(rt + ((lane & 31) * 8 + (lane >> 5) * 4) * 4) = u4{pk_h(kacc[1], kacc[0]), pk_h(kacc[2], kacc[1]), pk_h(kacc[3], kacc[2]), pk_h(kacc[13], kacc[3])};
        {
            const float n1_ = dppf<0x130>(kacc[4]);
            const float n2 = dppo<0x130>(dppf<0x134>(kacc[10]), kacc[8]);
            const float n3 = dppo<0x130>(dppf<0x134>(kacc[11]), kacc[10]);
            const float n4 = dppo<0x130>(dppf<0x134>(kacc[12]), kacc[11]);
            u4* r = (u4*)(rt + lane * 32);
            if (lane < 32) {
                const u32 p44 = pk_h(kacc[6], kacc[6]);
                r[64] = u4{pk_h(kacc[4], kacc[4]), pk_h(kacc[5], kacc[4]), pk_h(kacc[6], kacc[5]), p44};
                r[65] = u4{p44, pk_h(kacc[7], kacc[6]), pk_h(n1_, kacc[7]), pk_h(n1_, n1_)};
            }
            { const u32 pp = pk_h(kacc[8], kacc[8]), qq = pk_h(kacc[9], kacc[9]), nn = pk_h(n2, n2);
              r[128] = u4{pp, pp, pk_h(kacc[9], kacc[8]), qq}; r[129] = u4{qq, pk_h(n2, kacc[9]), nn, nn}; }
            { const float P = kacc[10], M = (lane & 1) ? n3 : P; const u32 pp = pk_h(P, P), nn = pk_h(n3, n3);
              r[256] = u4{pp, pp, pp, pk_h(M, P)}; r[257] = u4{pk_h(n3, M), nn, nn, nn}; }
            { const float P = kacc[11], M = (lane & 1) ? n4 : P; const u32 pp = pk_h(P, P), nn = pk_h(n4, n4);
              r[384] = u4{pp, pp, pp, pk_h(M, P)}; r[385] = u4{pk_h(n4, M), nn, nn, nn}; }
        }
        STAMP(3);
        const int r = lane & 15, g = lane >> 4;
        const u32 pa = lds0 + w * CV_R + 64 + (r + 9 - 8 * g) * 4;
        const u32 pb = lds0 + CV_U0 + w * CV_U + 512 + (16 * r - 16 + 8 * g) * 2 - 448;
        u4 wl[3];
        __builtin_amdgcn_s_setprio(KPRIO);
        {
            f4 acc[4];
            const u32 py = lds0 + CV_Y1 + w * 2048 + (16 * r + 4 * g) * 2;
            const f2 ys2 = {yscale, yscale};
            static_assert(cK == 0 && cU1 == 1 && cU2 == 2 && cY1 == 3, "counter numbers are built into the generated code");
    asm volatile(
        "v_add_u32 v72, 0, %7\n"
        "v_add_u32 v73, 1024, %7\n"
        "v_add_u32 v74, 2048, %7\n"
        "v_add_u32 v75, 3072, %7\n"
        "v_add_u32 v76, 4096, %7\n"
        "v_add_u32 v77, 5120, %7\n"
        "v_add_u32 v78, 6144, %7\n"
        "v_add_u32 v79, 7168, %7\n"
        "ds_read2_b32 v[128:129], v72 offset0:6 offset1:4\n"
        "ds_read2_b32 v[130:131], v72 offset0:2 offset1:0\n"
        "ds_read2_b32 v[132:133], v73 offset0:6 offset1:4\n"
        "ds_read2_b32 v[134:135], v73 offset0:2 offset1:0\n"
        "ds_read2_b32 v[136:137], v74 offset0:6 offset1:4\n"
        "ds_read2_b32 v[138:139], v74 offset0:2 offset1:0\n"
        "ds_read2_b32 v[140:141], v75 offset0:6 offset1:4\n"
        "ds_read2_b32 v[142:143], v75 offset0:2 offset1:0\n"
        "ds_read2_b32 v[144:145], v72 offset0:38 offset1:36\n"
        "ds_read2_b32 v[146:147], v72 offset0:34 offset1:32\n"
        "ds_read2_b32 v[148:149], v73 offset0:38 offset1:36\n"
        "ds_read2_b32 v[150:151], v73 offset0:34 offset1:32\n"
        "ds_read2_b32 v[152:153], v74 offset0:38 offset1:36\n"
        "ds_read2_b32 v[154:155], v74 offset0:34 offset1:32\n"
        "ds_read2_b32 v[156:157], v75 offset0:38 offset1:36\n"
        "ds_read2_b32 v[158:159], v75 offset0:34 offset1:32\n"
        "s_mov_b32 s41, 0x400000\n"
        ".Lcsa%=:\n"
        "ds_read_b32 v80, %11 offset:4\n"
        "s_waitcnt lgkmcnt(0)\n"
        "v_readfirstlane_b32 s40, v80\n"
        "s_cmp_ge_u32 s40, 2\n"
        "s_cbranch_scc1 .Lcea%=\n"
        "s_sub_u32 s41, s41, 1\n"
        "s_cbranch_scc0 .Lcsa%=\n"
        ".Lcea%=:\n"
        "ds_read_b128 v[108:111], %8 offset:1984\n"
        "ds_read_b128 v[104:107], %8 offset:1472\n"
        "ds_read_b128 v[100:103], %8 offset:960\n"
        "ds_read_b128 v[96:99], %8 offset:448\n"
        "ds_read_b128 v[124:127], %8 offset:1920\n"
        "ds_read_b128 v[120:123], %8 offset:1408\n"
        "ds_read_b128 v[116:119], %8 offset:896\n"
        "ds_read_b128 v[112:115], %8 offset:384\n"
        "s_waitcnt lgkmcnt(7)\n"
        "v_mfma_f32_16x16x32_f16 v[68:71], v[128:131], v[108:111], 0\n"
        "ds_read2_b32 v[160:161], v72 offset0:70 offset1:68\n"
        "ds_read2_b32 v[162:163], v72 offset0:66 offset1:64\n"
        "s_waitcnt lgkmcnt(8)\n"
        "v_mfma_f32_16x16x32_f16 v[64:67], v[128:131], v[104:107], 0\n"
        "ds_read_b128 v[108:111], %8 offset:1856\n"
        "ds_read2_b32 v[164:165], v73 offset0:70 offset1:68\n"
        "v_mfma_f32_16x16x32_f16 v[68:71], v[132:135], v[104:107], v[68:71]\n"
        "ds_read2_b32 v[166:167], v73 offset0:66 offset1:64\n"
        "s_waitcnt lgkmcnt(10)\n"
        "v_mfma_f32_16x16x32_f16 v[60:63], v[128:131], v[100:103], 0\n"
        "ds_read_b128 v[104:107], %8 offset:1344\n"
        "v_mfma_f32_16x16x32_f16 v[64:67], v[132:135], v[100:103], v[64:67]\n"
        "ds_read2_b32 v[168:169], v74 offset0:70 offset1:68\n"
        "v_mfma_f32_16x16x32_f16 v[68:71], v[136:139], v[100:103], v[68:71]\n"
        "ds_read2_b32 v[170:171], v74 offset0:66 offset1:64\n"
        "s_waitcnt lgkmcnt(8)\n"
        "v_mfma_f32_16x16x32_f16 v[56:59], v[128:131], v[96:99], 0\n"
        "ds_read_b128 v[100:103], %8 offset:832\n"
        "v_mfma_f32_16x16x32_f16 v[60:63], v[132:135], v[96:99], v[60:63]\n"
        "ds_read2_b32 v[172:173], v75 offset0:70 offset1:68\n"
        "v_mfma_f32_16x16x32_f16 v[64:67], v[136:139], v[96:99], v[64:67]\n"
        "ds_read2_b32 v[174:175], v75 offset0:66 offset1:64\n"
        "v_mfma_f32_16x16x32_f16 v[68:71], v[140:143], v[96:99], v[68:71]\n"
        "ds_read2_b32 v[176:177], v72 offset0:102 offset1:100\n"
        "v_mfma_f32_16x16x32_f16 v[68:71], v[144:147], v[124:127], v[68:71]\n"
        "ds_read_b128 v[96:99], %8 offset:320\n"
        "ds_read2_b32 v[178:179], v72 offset0:98 offset1:96\n"
        "v_mfma_f32_16x16x32_f16 v[64:67], v[144:147], v[120:123], v[64:67]\n"
        "ds_read_b128 v[124:127], %8 offset:1792\n"
        "ds_read2_b32 v[180:181], v73 offset0:102 offset1:100\n"
        "v_mfma_f32_16x16x32_f16 v[68:71], v[148:151], v[120:123], v[68:71]\n"
        "ds_read2_b32 v[182:183], v73 offset0:98 offset1:96\n"
        "v_mfma_f32_16x16x32_f16 v[60:63], v[144:147], v[116:119], v[60:63]\n"
        "ds_read_b128 v[120:123], %8 offset:1280\n"
        "v_mfma_f32_16x16x32_f16 v[64:67], v[148:151], v[116:119], v[64:67]\n"
        "ds_read2_b32 v[184:185], v74 offset0:102 offset1:100\n"
        "v_mfma_f32_16x16x32_f16 v[68:71], v[152:155], v[116:119], v[68:71]\n"
        "ds_read2_b32 v[186:187], v74 offset0:98 offset1:96\n"
        "v_mfma_f32_16x16x32_f16 v[56:59], v[144:147], v[112:115], v[56:59]\n"
        "ds_read_b128 v[116:119], %8 offset:768\n"
        "v_mfma_f32_16x16x32_f16 v[60:63], v[148:151], v[112:115], v[60:63]\n"
        "ds_read2_b32 v[188:189], v75 offset0:102 offset1:100\n"
        "v_mfma_f32_16x16x32_f16 v[64:67], v[152:155], v[112:115], v[64:67]\n"
        "ds_read2_b32 v[190:191], v75 offset0:98 offset1:96\n"
        "v_mfma_f32_16x16x32_f16 v[68:71], v[156:159], v[112:115], v[68:71]\n"
        "ds_read2_b32 v[192:193], v72 offset0:134 offset1:132\n"
        "s_waitcnt lgkmcnt(7)\n"
        "v_mfma_f32_16x16x32_f16 v[68:71], v[160:163], v[108:111], v[68:71]\n"
        "ds_read_b128 v[112:115], %8 offset:256\n"
        "ds_read2_b32 v[194:195], v72 offset0:130 offset1:128\n"
        "v_mfma_f32_16x16x32_f16 v[64:67], v[160:163], v[104:107], v[64:67]\n"
        "ds_read_b128 v[108:111], %8 offset:1728\n"
        "ds_read2_b32 v[196:197], v73 offset0:134 offset1:132\n"
        "v_mfma_f32_16x16x32_f16 v[68:71], v[164:167], v[104:107], v[68:71]\n"
        "ds_read2_b32 v[198:199], v73 offset0:130 offset1:128\n"
        "v_mfma_f32_16x16x32_f16 v[60:63], v[160:163], v[100:103], v[60:63]\n"
        "ds_read_b128 v[104:107], %8 offset:1216\n"
        "v_mfma_f32_16x16x32_f16 v[64:67], v[164:167], v[100:103], v[64:67]\n"
        "ds_read2_b32 v[200:201], v74 offset0:134 offset1:132\n"
        "v_mfma_f32_16x16x32_f16 v[68:71], v[168:171], v[100:103], v[68:71]\n"
        "ds_read2_b32 v[202:203], v74 offset0:130 offset1:128\n"
        "v_mfma_f32_16x16x32_f16 v[56:59], v[160:163], v[96:99], v[56:59]\n"
        "ds_read_b128 v[100:103], %8 offset:704\n"
        "v_mfma_f32_16x16x32_f16 v[60:63], v[164:167], v[96:99], v[60:63]\n"
        "ds_read2_b32 v[204:205], v75 offset0:134 offset1:132\n"
        "v_mfma_f32_16x16x32_f16 v[64:67], v[168:171], v[96:99], v[64:67]\n"
        "ds_read2_b32 v[206:207], v75 offset0:130 offset1:128\n"
        "v_mfma_f32_16x16x32_f16 v[68:71], v[172:175], v[96:99], v[68:71]\n"
        "ds_read2_b32 v[208:209], v72 offset0:166 offset1:164\n"
        "v_mfma_f32_16x16x32_f16 v[68:71], v[176:179], v[124:127], v[68:71]\n"
        "ds_read_b128 v[96:99], %8 offset:192\n"
        "ds_read2_b32 v[210:211], v72 offset0:162 offset1:160\n"
        "s_waitcnt lgkmcnt(8)\n"
        "v_mfma_f32_16x16x32_f16 v[64:67], v[176:179], v[120:123], v[64:67]\n"
        "ds_read_b128 v[124:127], %8 offset:1664\n"
        "ds_read2_b32 v[212:213], v73 offset0:166 offset1:164\n"
        "v_mfma_f32_16x16x32_f16 v[68:71], v[180:183], v[120:123], v[68:71]\n"
        "ds_read2_b32 v[214:215], v73 offset0:162 offset1:160\n"
        "v_mfma_f32_16x16x32_f16 v[60:63], v[176:179], v[116:119], v[60:63]\n"
        "ds_read_b128 v[120:123], %8 offset:1152\n"
        "v_mfma_f32_16x16x32_f16 v[64:67], v[180:183], v[116:119], v[64:67]\n"
        "ds_read2_b32 v[216:217], v74 offset0:166 offset1:164\n"
        "v_mfma_f32_16x16x32_f16 v[68:71], v[184:187], v[116:119], v[68:71]\n"
        "ds_read2_b32 v[218:219], v74 offset0:162 offset1:160\n"
        "v_mfma_f32_16x16x32_f16 v[56:59], v[176:179], v[112:115], v[56:59]\n"
        "ds_read_b128 v[116:119], %8 offset:640\n"
        "v_mfma_f32_16x16x32_f16 v[60:63], v[180:183], v[112:115], v[60:63]\n"
        "ds_read2_b32 v[220:221], v75 offset0:166 offset1:164\n"
        "v_mfma_f32_16x16x32_f16 v[64:67], v[184:187], v[112:115], v[64:67]\n"
        "ds_read2_b32 v[222:223], v75 offset0:162 offset1:160\n"
        "v_mfma_f32_16x16x32_f16 v[68:71], v[188:191], v[112:115], v[68:71]\n"
        "ds_read2_b32 v[224:225], v72 offset0:198 offset1:196\n"
        "v_mfma_f32_16x16x32_f16 v[68:71], v[192:195], v[108:111], v[68:71]\n"
        "ds_read_b128 v[112:115], %8 offset:128\n"
        "ds_read2_b32 v[226:227], v72 offset0:194 offset1:192\n"
        "v_mfma_f32_16x16x32_f16 v[64:67], v[192:195], v[104:107], v[64:67]\n"
        "ds_read_b128 v[108:111], %8 offset:1600\n"
        "ds_read2_b32 v[228:229], v73 offset0:198 offset1:196\n"
        "v_mfma_f32_16x16x32_f16 v[68:71], v[196:199], v[104:107], v[68:71]\n"
        "ds_read2_b32 v[230:231], v73 offset0:194 offset1:192\n"
        "s_waitcnt lgkmcnt(9)\n"
        "v_mfma_f32_16x16x32_f16 v[60:63], v[192:195], v[100:103], v[60:63]\n"
        "ds_read_b128 v[104:107], %8 offset:1088\n"
        "v_mfma_f32_16x16x32_f16 v[64:67], v[196:199], v[100:103], v[64:67]\n"
        "ds_read2_b32 v[232:233], v74 offset0:198 offset1:196\n"
        "v_mfma_f32_16x16x32_f16 v[68:71], v[200:203], v[100:103], v[68:71]\n"
        "ds_read2_b32 v[234:235], v74 offset0:194 offset1:192\n"
        "v_mfma_f32_16x16x32_f16 v[56:59], v[192:195], v[96:99], v[56:59]\n"
        "ds_read_b128 v[100:103], %8 offset:576\n"
        "v_mfma_f32_16x16x32_f16 v[60:63], v[196:199], v[96:99], v[60:63]\n"
        "ds_read2_b32 v[236:237], v75 offset0:198 offset1:196\n"
        "v_mfma_f32_16x16x32_f16 v[64:67], v[200:203], v[96:99], v[64:67]\n"
        "ds_read2_b32 v[238:239], v75 offset0:194 offset1:192\n"
        "v_mfma_f32_16x16x32_f16 v[68:71], v[204:207], v[96:99], v[68:71]\n"
        "ds_read2_b32 v[240:241], v72 offset0:230 offset1:228\n"
        "v_mfma_f32_16x16x32_f16 v[68:71], v[208:211], v[124:127], v[68:71]\n"
        "ds_read_b128 v[96:99], %8 offset:64\n"
        "ds_read2_b32 v[242:243], v72 offset0:226 offset1:224\n"
        "v_mfma_f32_16x16x32_f16 v[64:67], v[208:211], v[120:123], v[64:67]\n"
        "ds_read_b128 v[124:127], %8 offset:1536\n"
        "ds_read2_b32 v[244:245], v73 offset0:230 offset1:228\n"
        "v_mfma_f32_16x16x32_f16 v[68:71], v[212:215], v[120:123], v[68:71]\n"
        "ds_read2_b32 v[246:247], v73 offset0:226 offset1:224\n"
        "s_waitcnt lgkmcnt(9)\n"
        "v_mfma_f32_16x16x32_f16 v[60:63], v[208:211], v[116:119], v[60:63]\n"
        "ds_read_b128 v[120:123], %8 offset:1024\n"
        "v_mfma_f32_16x16x32_f16 v[64:67], v[212:215], v[116:119], v[64:67]\n"
        "ds_read2_b32 v[248:249], v74 offset0:230 offset1:228\n"
        "v_mfma_f32_16x16x32_f16 v[68:71], v[216:219], v[116:119], v[68:71]\n"
        "ds_read2_b32 v[250:251], v74 offset0:226 offset1:224\n"
        "v_mfma_f32_16x16x32_f16 v[56:59], v[208:211], v[112:115], v[56:59]\n"
        "ds_read_b128 v[116:119], %8 offset:512\n"
        "v_mfma_f32_16x16x32_f16 v[60:63], v[212:215], v[112:115], v[60:63]\n"
        "ds_read2_b32 v[252:253], v75 offset0:230 offset1:228\n"
        "v_mfma_f32_16x16x32_f16 v[64:67], v[216:219], v[112:115], v[64:67]\n"
        "ds_read2_b32 v[254:255], v75 offset0:226 offset1:224\n"
        "v_mfma_f32_16x16x32_f16 v[68:71], v[220:223], v[112:115], v[68:71]\n"
        "v_mfma_f32_16x16x32_f16 v[68:71], v[224:227], v[108:111], v[68:71]\n"
        "ds_read_b128 v[112:115], %8 offset:0\n"
        "v_mfma_f32_16x16x32_f16 v[64:67], v[224:227], v[104:107], v[64:67]\n"
        "v_mfma_f32_16x16x32_f16 v[68:71], v[228:231], v[104:107], v[68:71]\n"
        "s_waitcnt lgkmcnt(4)\n"
        "v_mfma_f32_16x16x32_f16 v[60:63], v[224:227], v[100:103], v[60:63]\n"
        "v_mfma_f32_16x16x32_f16 v[64:67], v[228:231], v[100:103], v[64:67]\n"
        "v_mfma_f32_16x16x32_f16 v[68:71], v[232:235], v[100:103], v[68:71]\n"
        "v_mfma_f32_16x16x32_f16 v[56:59], v[224:227], v[96:99], v[56:59]\n"
        "v_mfma_f32_16x16x32_f16 v[60:63], v[228:231], v[96:99], v[60:63]\n"
        "v_mfma_f32_16x16x32_f16 v[64:67], v[232:235], v[96:99], v[64:67]\n"
        "v_mfma_f32_16x16x32_f16 v[68:71], v[236:239], v[96:99], v[68:71]\n"
        "v_mfma_f32_16x16x32_f16 v[68:71], v[240:243], v[124:127], v[68:71]\n"
        "v_mfma_f32_16x16x32_f16 v[64:67], v[240:243], v[120:123], v[64:67]\n"
        "v_mfma_f32_16x16x32_f16 v[68:71], v[244:247], v[120:123], v[68:71]\n"
        "s_waitcnt lgkmcnt(0)\n"
        "v_mfma_f32_16x16x32_f16 v[60:63], v[240:243], v[116:119], v[60:63]\n"
        "v_mfma_f32_16x16x32_f16 v[64:67], v[244:247], v[116:119], v[64:67]\n"
        "v_mfma_f32_16x16x32_f16 v[68:71], v[248:251], v[116:119], v[68:71]\n"
        "v_mfma_f32_16x16x32_f16 v[56:59], v[240:243], v[112:115], v[56:59]\n"
        "v_mfma_f32_16x16x32_f16 v[60:63], v[244:247], v[112:115], v[60:63]\n"
        "v_mfma_f32_16x16x32_f16 v[64:67], v[248:251], v[112:115], v[64:67]\n"
        "v_mfma_f32_16x16x32_f16 v[68:71], v[252:255], v[112:115], v[68:71]\n"
        "s_mov_b32 s41, 0x400000\n"
        ".Lcsk%=:\n"
        "ds_read_b32 v80, %11 offset:0\n"
        "s_waitcnt lgkmcnt(0)\n"
        "v_readfirstlane_b32 s40, v80\n"
        "s_cmp_ge_u32 s40, 4\n"
        "s_cbranch_scc1 .Lcek%=\n"
        "s_sub_u32 s41, s41, 1\n"
        "s_cbranch_scc0 .Lcsk%=\n"
        ".Lcek%=:\n"
        "v_pk_mul_f32 v[56:57], v[56:57], %10\n"
        "v_pk_mul_f32 v[58:59], v[58:59], %10\n"
        "v_pk_mul_f32 v[60:61], v[60:61], %10\n"
        "v_pk_mul_f32 v[62:63], v[62:63], %10\n"
        "v_pk_mul_f32 v[64:65], v[64:65], %10\n"
        "v_pk_mul_f32 v[66:67], v[66:67], %10\n"
        "v_pk_mul_f32 v[68:69], v[68:69], %10\n"
        "v_pk_mul_f32 v[70:71], v[70:71], %10\n"
        "v_cvt_pk_bf16_f32 v56, v56, v57\n"
        "v_cvt_pk_bf16_f32 v57, v58, v59\n"
        "v_cvt_pk_bf16_f32 v60, v60, v61\n"
        "v_cvt_pk_bf16_f32 v61, v62, v63\n"
        "v_cvt_pk_bf16_f32 v64, v64, v65\n"
        "v_cvt_pk_bf16_f32 v65, v66, v67\n"
        "v_cvt_pk_bf16_f32 v68, v68, v69\n"
        "v_cvt_pk_bf16_f32 v69, v70, v71\n"
        "ds_write_b64 %9, v[56:57] offset:0\n"
        "ds_write_b64 %9, v[60:61] offset:512\n"
        "ds_write_b64 %9, v[64:65] offset:1024\n"
        "ds_write_b64 %9, v[68:69] offset:1536\n"
        "s_mov_b64 exec, 1\n"
        "ds_add_u32 %11, %12 offset:12\n"
        "s_mov_b64 exec, -1\n"
        "global_load_dwordx4 %4, %13, %16\n"
        "global_load_dwordx4 %5, %14, %16\n"
        "global_load_dwordx4 %6, %15, %16\n"
        "s_mov_b32 s41, 0x400000\n"
        ".Lcsb%=:\n"
        "ds_read_b32 v80, %11 offset:8\n"
        "s_waitcnt lgkmcnt(0)\n"
        "v_readfirstlane_b32 s40, v80\n"
        "s_cmp_ge_u32 s40, 2\n"
        "s_cbranch_scc1 .Lceb%=\n"
        "s_sub_u32 s41, s41, 1\n"
        "s_cbranch_scc0 .Lcsb%=\n"
        ".Lceb%=:\n"
        "ds_read_b128 v[124:127], %8 offset:4032\n"
        "ds_read_b128 v[120:123], %8 offset:3520\n"
        "ds_read_b128 v[116:119], %8 offset:3008\n"
        "ds_read_b128 v[112:115], %8 offset:2496\n"
        "ds_read_b128 v[108:111], %8 offset:1984\n"
        "ds_read_b128 v[104:107], %8 offset:1472\n"
        "ds_read_b128 v[100:103], %8 offset:960\n"
        "ds_read_b128 v[96:99], %8 offset:448\n"
        "s_waitcnt lgkmcnt(7)\n"
        "v_mfma_f32_16x16x32_f16 %3, v[128:131], v[124:127], 0\n"
        "ds_read2_b32 v[80:81], v76 offset0:6 offset1:4\n"
        "s_waitcnt lgkmcnt(7)\n"
        "v_mfma_f32_16x16x32_f16 %2, v[128:131], v[120:123], 0\n"
        "ds_read2_b32 v[82:83], v76 offset0:2 offset1:0\n"
        "v_mfma_f32_16x16x32_f16 %3, v[132:135], v[120:123], %3\n"
        "ds_read2_b32 v[84:85], v77 offset0:6 offset1:4\n"
        "s_waitcnt lgkmcnt(8)\n"
        "v_mfma_f32_16x16x32_f16 %1, v[128:131], v[116:119], 0\n"
        "ds_read2_b32 v[86:87], v77 offset0:2 offset1:0\n"
        "v_mfma_f32_16x16x32_f16 %2, v[132:135], v[116:119], %2\n"
        "ds_read2_b32 v[88:89], v78 offset0:6 offset1:4\n"
        "v_mfma_f32_16x16x32_f16 %3, v[136:139], v[116:119], %3\n"
        "ds_read2_b32 v[90:91], v78 offset0:2 offset1:0\n"
        "s_waitcnt lgkmcnt(6)\n"
        "v_mfma_f32_16x16x32_f16 %0, v[128:131], v[112:115], 0\n"
        "ds_read2_b32 v[92:93], v79 offset0:6 offset1:4\n"
        "v_mfma_f32_16x16x32_f16 %1, v[132:135], v[112:115], %1\n"
        "ds_read2_b32 v[94:95], v79 offset0:2 offset1:0\n"
        "v_mfma_f32_16x16x32_f16 %2, v[136:139], v[112:115], %2\n"
        "ds_read_b128 v[124:127], %8 offset:3968\n"
        "v_mfma_f32_16x16x32_f16 %3, v[140:143], v[112:115], %3\n"
        "ds_read_b128 v[120:123], %8 offset:3456\n"
        "v_mfma_f32_16x16x32_f16 %0, v[132:135], v[108:111], %0\n"
        "ds_read_b128 v[116:119], %8 offset:2944\n"
        "v_mfma_f32_16x16x32_f16 %1, v[136:139], v[108:111], %1\n"
        "ds_read_b128 v[112:115], %8 offset:2432\n"
        "v_mfma_f32_16x16x32_f16 %2, v[140:143], v[108:111], %2\n"
        "s_waitcnt lgkmcnt(6)\n"
        "v_mfma_f32_16x16x32_f16 %3, v[80:83], v[108:111], %3\n"
        "v_mfma_f32_16x16x32_f16 %0, v[136:139], v[104:107], %0\n"
        "ds_read_b128 v[108:111], %8 offset:1920\n"
        "v_mfma_f32_16x16x32_f16 %1, v[140:143], v[104:107], %1\n"
        "v_mfma_f32_16x16x32_f16 %2, v[80:83], v[104:107], %2\n"
        "v_mfma_f32_16x16x32_f16 %3, v[84:87], v[104:107], %3\n"
        "v_mfma_f32_16x16x32_f16 %0, v[140:143], v[100:103], %0\n"
        "ds_read_b128 v[104:107], %8 offset:1408\n"
        "v_mfma_f32_16x16x32_f16 %1, v[80:83], v[100:103], %1\n"
        "v_mfma_f32_16x16x32_f16 %2, v[84:87], v[100:103], %2\n"
        "v_mfma_f32_16x16x32_f16 %3, v[88:91], v[100:103], %3\n"
        "v_mfma_f32_16x16x32_f16 %0, v[80:83], v[96:99], %0\n"
        "ds_read_b128 v[100:103], %8 offset:896\n"
        "v_mfma_f32_16x16x32_f16 %1, v[84:87], v[96:99], %1\n"
        "v_mfma_f32_16x16x32_f16 %2, v[88:91], v[96:99], %2\n"
        "s_waitcnt lgkmcnt(2)\n"
        "v_mfma_f32_16x16x32_f16 %3, v[92:95], v[96:99], %3\n"
        "v_mfma_f32_16x16x32_f16 %3, v[144:147], v[124:127], %3\n"
        "ds_read_b128 v[96:99], %8 offset:384\n"
        "v_mfma_f32_16x16x32_f16 %2, v[144:147], v[120:123], %2\n"
        "ds_read2_b32 v[80:81], v76 offset0:38 offset1:36\n"
        "v_mfma_f32_16x16x32_f16 %3, v[148:151], v[120:123], %3\n"
        "ds_read2_b32 v[82:83], v76 offset0:34 offset1:32\n"
        "v_mfma_f32_16x16x32_f16 %1, v[144:147], v[116:119], %1\n"
        "ds_read2_b32 v[84:85], v77 offset0:38 offset1:36\n"
        "v_mfma_f32_16x16x32_f16 %2, v[148:151], v[116:119], %2\n"
        "ds_read2_b32 v[86:87], v77 offset0:34 offset1:32\n"
        "v_mfma_f32_16x16x32_f16 %3, v[152:155], v[116:119], %3\n"
        "ds_read2_b32 v[88:89], v78 offset0:38 offset1:36\n"
        "v_mfma_f32_16x16x32_f16 %0, v[144:147], v[112:115], %0\n"
        "ds_read2_b32 v[90:91], v78 offset0:34 offset1:32\n"
        "v_mfma_f32_16x16x32_f16 %1, v[148:151], v[112:115], %1\n"
        "ds_read2_b32 v[92:93], v79 offset0:38 offset1:36\n"
        "v_mfma_f32_16x16x32_f16 %2, v[152:155], v[112:115], %2\n"
        "ds_read2_b32 v[94:95], v79 offset0:34 offset1:32\n"
        "v_mfma_f32_16x16x32_f16 %3, v[156:159], v[112:115], %3\n"
        "ds_read_b128 v[124:127], %8 offset:3904\n"
        "v_mfma_f32_16x16x32_f16 %0, v[148:151], v[108:111], %0\n"
        "ds_read_b128 v[120:123], %8 offset:3392\n"
        "v_mfma_f32_16x16x32_f16 %1, v[152:155], v[108:111], %1\n"
        "ds_read_b128 v[116:119], %8 offset:2880\n"
        "v_mfma_f32_16x16x32_f16 %2, v[156:159], v[108:111], %2\n"
        "ds_read_b128 v[112:115], %8 offset:2368\n"
        "s_waitcnt lgkmcnt(7)\n"
        "v_mfma_f32_16x16x32_f16 %3, v[80:83], v[108:111], %3\n"
        "v_mfma_f32_16x16x32_f16 %0, v[152:155], v[104:107], %0\n"
        "ds_read_b128 v[108:111], %8 offset:1856\n"
        "v_mfma_f32_16x16x32_f16 %1, v[156:159], v[104:107], %1\n"
        "v_mfma_f32_16x16x32_f16 %2, v[80:83], v[104:107], %2\n"
        "v_mfma_f32_16x16x32_f16 %3, v[84:87], v[104:107], %3\n"
        "v_mfma_f32_16x16x32_f16 %0, v[156:159], v[100:103], %0\n"
        "ds_read_b128 v[104:107], %8 offset:1344\n"
        "v_mfma_f32_16x16x32_f16 %1, v[80:83], v[100:103], %1\n"
        "v_mfma_f32_16x16x32_f16 %2, v[84:87], v[100:103], %2\n"
        "s_waitcnt lgkmcnt(2)\n"
        "v_mfma_f32_16x16x32_f16 %3, v[88:91], v[100:103], %3\n"
        "v_mfma_f32_16x16x32_f16 %0, v[80:83], v[96:99], %0\n"
        "ds_read_b128 v[100:103], %8 offset:832\n"
        "v_mfma_f32_16x16x32_f16 %1, v[84:87], v[96:99], %1\n"
        "v_mfma_f32_16x16x32_f16 %2, v[88:91], v[96:99], %2\n"
        "v_mfma_f32_16x16x32_f16 %3, v[92:95], v[96:99], %3\n"
        "v_mfma_f32_16x16x32_f16 %3, v[160:163], v[124:127], %3\n"
        "ds_read_b128 v[96:99], %8 offset:320\n"
        "v_mfma_f32_16x16x32_f16 %2, v[160:163], v[120:123], %2\n"
        "ds_read2_b32 v[80:81], v76 offset0:70 offset1:68\n"
        "v_mfma_f32_16x16x32_f16 %3, v[164:167], v[120:123], %3\n"
        "ds_read2_b32 v[82:83], v76 offset0:66 offset1:64\n"
        "v_mfma_f32_16x16x32_f16 %1, v[160:163], v[116:119], %1\n"
        "ds_read2_b32 v[84:85], v77 offset0:70 offset1:68\n"
        "v_mfma_f32_16x16x32_f16 %2, v[164:167], v[116:119], %2\n"
        "ds_read2_b32 v[86:87], v77 offset0:66 offset1:64\n"
        "v_mfma_f32_16x16x32_f16 %3, v[168:171], v[116:119], %3\n"
        "ds_read2_b32 v[88:89], v78 offset0:70 offset1:68\n"
        "v_mfma_f32_16x16x32_f16 %0, v[160:163], v[112:115], %0\n"
        "ds_read2_b32 v[90:91], v78 offset0:66 offset1:64\n"
        "v_mfma_f32_16x16x32_f16 %1, v[164:167], v[112:115], %1\n"
        "ds_read2_b32 v[92:93], v79 offset0:70 offset1:68\n"
        "v_mfma_f32_16x16x32_f16 %2, v[168:171], v[112:115], %2\n"
        "ds_read2_b32 v[94:95], v79 offset0:66 offset1:64\n"
        "v_mfma_f32_16x16x32_f16 %3, v[172:175], v[112:115], %3\n"
        "ds_read_b128 v[124:127], %8 offset:3840\n"
        "s_waitcnt lgkmcnt(7)\n"
        "v_mfma_f32_16x16x32_f16 %0, v[164:167], v[108:111], %0\n"
        "ds_read_b128 v[120:123], %8 offset:3328\n"
        "v_mfma_f32_16x16x32_f16 %1, v[168:171], v[108:111], %1\n"
        "ds_read_b128 v[116:119], %8 offset:2816\n"
        "v_mfma_f32_16x16x32_f16 %2, v[172:175], v[108:111], %2\n"
        "ds_read_b128 v[112:115], %8 offset:2304\n"
        "v_mfma_f32_16x16x32_f16 %3, v[80:83], v[108:111], %3\n"
        "v_mfma_f32_16x16x32_f16 %0, v[168:171], v[104:107], %0\n"
        "ds_read_b128 v[108:111], %8 offset:1792\n"
        "v_mfma_f32_16x16x32_f16 %1, v[172:175], v[104:107], %1\n"
        "v_mfma_f32_16x16x32_f16 %2, v[80:83], v[104:107], %2\n"
        "s_waitcnt lgkmcnt(4)\n"
        "v_mfma_f32_16x16x32_f16 %3, v[84:87], v[104:107], %3\n"
        "v_mfma_f32_16x16x32_f16 %0, v[172:175], v[100:103], %0\n"
        "ds_read_b128 v[104:107], %8 offset:1280\n"
        "v_mfma_f32_16x16x32_f16 %1, v[80:83], v[100:103], %1\n"
        "v_mfma_f32_16x16x32_f16 %2, v[84:87], v[100:103], %2\n"
        "v_mfma_f32_16x16x32_f16 %3, v[88:91], v[100:103], %3\n"
        "v_mfma_f32_16x16x32_f16 %0, v[80:83], v[96:99], %0\n"
        "ds_read_b128 v[100:103], %8 offset:768\n"
        "v_mfma_f32_16x16x32_f16 %1, v[84:87], v[96:99], %1\n"
        "v_mfma_f32_16x16x32_f16 %2, v[88:91], v[96:99], %2\n"
        "v_mfma_f32_16x16x32_f16 %3, v[92:95], v[96:99], %3\n"
        "v_mfma_f32_16x16x32_f16 %3, v[176:179], v[124:127], %3\n"
        "ds_read_b128 v[96:99], %8 offset:256\n"
        "s_waitcnt lgkmcnt(2)\n"
        "v_mfma_f32_16x16x32_f16 %2, v[176:179], v[120:123], %2\n"
        "ds_read2_b32 v[80:81], v76 offset0:102 offset1:100\n"
        "v_mfma_f32_16x16x32_f16 %3, v[180:183], v[120:123], %3\n"
        "ds_read2_b32 v[82:83], v76 offset0:98 offset1:96\n"
        "v_mfma_f32_16x16x32_f16 %1, v[176:179], v[116:119], %1\n"
        "ds_read2_b32 v[84:85], v77 offset0:102 offset1:100\n"
        "v_mfma_f32_16x16x32_f16 %2, v[180:183], v[116:119], %2\n"
        "ds_read2_b32 v[86:87], v77 offset0:98 offset1:96\n"
        "v_mfma_f32_16x16x32_f16 %3, v[184:187], v[116:119], %3\n"
        "ds_read2_b32 v[88:89], v78 offset0:102 offset1:100\n"
        "v_mfma_f32_16x16x32_f16 %0, v[176:179], v[112:115], %0\n"
        "ds_read2_b32 v[90:91], v78 offset0:98 offset1:96\n"
        "v_mfma_f32_16x16x32_f16 %1, v[180:183], v[112:115], %1\n"
        "ds_read2_b32 v[92:93], v79 offset0:102 offset1:100\n"
        "v_mfma_f32_16x16x32_f16 %2, v[184:187], v[112:115], %2\n"
        "ds_read2_b32 v[94:95], v79 offset0:98 offset1:96\n"
        "v_mfma_f32_16x16x32_f16 %3, v[188:191], v[112:115], %3\n"
        "ds_read_b128 v[124:127], %8 offset:3776\n"
        "v_mfma_f32_16x16x32_f16 %0, v[180:183], v[108:111], %0\n"
        "ds_read_b128 v[120:123], %8 offset:3264\n"
        "v_mfma_f32_16x16x32_f16 %1, v[184:187], v[108:111], %1\n"
        "ds_read_b128 v[116:119], %8 offset:2752\n"
        "v_mfma_f32_16x16x32_f16 %2, v[188:191], v[108:111], %2\n"
        "ds_read_b128 v[112:115], %8 offset:2240\n"
        "s_waitcnt lgkmcnt(7)\n"
        "v_mfma_f32_16x16x32_f16 %3, v[80:83], v[108:111], %3\n"
        "v_mfma_f32_16x16x32_f16 %0, v[184:187], v[104:107], %0\n"
        "ds_read_b128 v[108:111], %8 offset:1728\n"
        "v_mfma_f32_16x16x32_f16 %1, v[188:191], v[104:107], %1\n"
        "v_mfma_f32_16x16x32_f16 %2, v[80:83], v[104:107], %2\n"
        "v_mfma_f32_16x16x32_f16 %3, v[84:87], v[104:107], %3\n"
        "v_mfma_f32_16x16x32_f16 %0, v[188:191], v[100:103], %0\n"
        "ds_read_b128 v[104:107], %8 offset:1216\n"
        "v_mfma_f32_16x16x32_f16 %1, v[80:83], v[100:103], %1\n"
        "v_mfma_f32_16x16x32_f16 %2, v[84:87], v[100:103], %2\n"
        "s_waitcnt lgkmcnt(2)\n"
        "v_mfma_f32_16x16x32_f16 %3, v[88:91], v[100:103], %3\n"
        "v_mfma_f32_16x16x32_f16 %0, v[80:83], v[96:99], %0\n"
        "ds_read_b128 v[100:103], %8 offset:704\n"
        "v_mfma_f32_16x16x32_f16 %1, v[84:87], v[96:99], %1\n"
        "v_mfma_f32_16x16x32_f16 %2, v[88:91], v[96:99], %2\n"
        "v_mfma_f32_16x16x32_f16 %3, v[92:95], v[96:99], %3\n"
        "v_mfma_f32_16x16x32_f16 %3, v[192:195], v[124:127], %3\n"
        "ds_read_b128 v[96:99], %8 offset:192\n"
        "v_mfma_f32_16x16x32_f16 %2, v[192:195], v[120:123], %2\n"
        "ds_read2_b32 v[80:81], v76 offset0:134 offset1:132\n"
        "v_mfma_f32_16x16x32_f16 %3, v[196:199], v[120:123], %3\n"
        "ds_read2_b32 v[82:83], v76 offset0:130 offset1:128\n"
        "v_mfma_f32_16x16x32_f16 %1, v[192:195], v[116:119], %1\n"
        "ds_read2_b32 v[84:85], v77 offset0:134 offset1:132\n"
        "v_mfma_f32_16x16x32_f16 %2, v[196:199], v[116:119], %2\n"
        "ds_read2_b32 v[86:87], v77 offset0:130 offset1:128\n"
        "v_mfma_f32_16x16x32_f16 %3, v[200:203], v[116:119], %3\n"
        "ds_read2_b32 v[88:89], v78 offset0:134 offset1:132\n"
        "v_mfma_f32_16x16x32_f16 %0, v[192:195], v[112:115], %0\n"
        "ds_read2_b32 v[90:91], v78 offset0:130 offset1:128\n"
        "v_mfma_f32_16x16x32_f16 %1, v[196:199], v[112:115], %1\n"
        "ds_read2_b32 v[92:93], v79 offset0:134 offset1:132\n"
        "v_mfma_f32_16x16x32_f16 %2, v[200:203], v[112:115], %2\n"
        "ds_read2_b32 v[94:95], v79 offset0:130 offset1:128\n"
        "v_mfma_f32_16x16x32_f16 %3, v[204:207], v[112:115], %3\n"
        "ds_read_b128 v[124:127], %8 offset:3712\n"
        "s_waitcnt lgkmcnt(7)\n"
        "v_mfma_f32_16x16x32_f16 %0, v[196:199], v[108:111], %0\n"
        "ds_read_b128 v[120:123], %8 offset:3200\n"
        "v_mfma_f32_16x16x32_f16 %1, v[200:203], v[108:111], %1\n"
        "ds_read_b128 v[116:119], %8 offset:2688\n"
        "v_mfma_f32_16x16x32_f16 %2, v[204:207], v[108:111], %2\n"
        "ds_read_b128 v[112:115], %8 offset:2176\n"
        "v_mfma_f32_16x16x32_f16 %3, v[80:83], v[108:111], %3\n"
        "v_mfma_f32_16x16x32_f16 %0, v[200:203], v[104:107], %0\n"
        "ds_read_b128 v[108:111], %8 offset:1664\n"
        "v_mfma_f32_16x16x32_f16 %1, v[204:207], v[104:107], %1\n"
        "v_mfma_f32_16x16x32_f16 %2, v[80:83], v[104:107], %2\n"
        "s_waitcnt lgkmcnt(4)\n"
        "v_mfma_f32_16x16x32_f16 %3, v[84:87], v[104:107], %3\n"
        "v_mfma_f32_16x16x32_f16 %0, v[204:207], v[100:103], %0\n"
        "ds_read_b128 v[104:107], %8 offset:1152\n"
        "v_mfma_f32_16x16x32_f16 %1, v[80:83], v[100:103], %1\n"
        "v_mfma_f32_16x16x32_f16 %2, v[84:87], v[100:103], %2\n"
        "v_mfma_f32_16x16x32_f16 %3, v[88:91], v[100:103], %3\n"
        "v_mfma_f32_16x16x32_f16 %0, v[80:83], v[96:99], %0\n"
        "ds_read_b128 v[100:103], %8 offset:640\n"
        "v_mfma_f32_16x16x32_f16 %1, v[84:87], v[96:99], %1\n"
        "v_mfma_f32_16x16x32_f16 %2, v[88:91], v[96:99], %2\n"
        "v_mfma_f32_16x16x32_f16 %3, v[92:95], v[96:99], %3\n"
        "v_mfma_f32_16x16x32_f16 %3, v[208:211], v[124:127], %3\n"
        "ds_read_b128 v[96:99], %8 offset:128\n"
        "s_waitcnt lgkmcnt(2)\n"
        "v_mfma_f32_16x16x32_f16 %2, v[208:211], v[120:123], %2\n"
        "ds_read2_b32 v[80:81], v76 offset0:166 offset1:164\n"
        "v_mfma_f32_16x16x32_f16 %3, v[212:215], v[120:123], %3\n"
        "ds_read2_b32 v[82:83], v76 offset0:162 offset1:160\n"
        "v_mfma_f32_16x16x32_f16 %1, v[208:211], v[116:119], %1\n"
        "ds_read2_b32 v[84:85], v77 offset0:166 offset1:164\n"
        "v_mfma_f32_16x16x32_f16 %2, v[212:215], v[116:119], %2\n"
        "ds_read2_b32 v[86:87], v77 offset0:162 offset1:160\n"
        "v_mfma_f32_16x16x32_f16 %3, v[216:219], v[116:119], %3\n"
        "ds_read2_b32 v[88:89], v78 offset0:166 offset1:164\n"
        "v_mfma_f32_16x16x32_f16 %0, v[208:211], v[112:115], %0\n"
        "ds_read2_b32 v[90:91], v78 offset0:162 offset1:160\n"
        "v_mfma_f32_16x16x32_f16 %1, v[212:215], v[112:115], %1\n"
        "ds_read2_b32 v[92:93], v79 offset0:166 offset1:164\n"
        "v_mfma_f32_16x16x32_f16 %2, v[216:219], v[112:115], %2\n"
        "ds_read2_b32 v[94:95], v79 offset0:162 offset1:160\n"
        "v_mfma_f32_16x16x32_f16 %3, v[220:223], v[112:115], %3\n"
        "ds_read_b128 v[124:127], %8 offset:3648\n"
        "v_mfma_f32_16x16x32_f16 %0, v[212:215], v[108:111], %0\n"
        "ds_read_b128 v[120:123], %8 offset:3136\n"
        "v_mfma_f32_16x16x32_f16 %1, v[216:219], v[108:111], %1\n"
        "ds_read_b128 v[116:119], %8 offset:2624\n"
        "v_mfma_f32_16x16x32_f16 %2, v[220:223], v[108:111], %2\n"
        "ds_read_b128 v[112:115], %8 offset:2112\n"
        "s_waitcnt lgkmcnt(7)\n"
        "v_mfma_f32_16x16x32_f16 %3, v[80:83], v[108:111], %3\n"
        "v_mfma_f32_16x16x32_f16 %0, v[216:219], v[104:107], %0\n"
        "ds_read_b128 v[108:111], %8 offset:1600\n"
        "v_mfma_f32_16x16x32_f16 %1, v[220:223], v[104:107], %1\n"
        "v_mfma_f32_16x16x32_f16 %2, v[80:83], v[104:107], %2\n"
        "v_mfma_f32_16x16x32_f16 %3, v[84:87], v[104:107], %3\n"
        "v_mfma_f32_16x16x32_f16 %0, v[220:223], v[100:103], %0\n"
        "ds_read_b128 v[104:107], %8 offset:1088\n"
        "v_mfma_f32_16x16x32_f16 %1, v[80:83], v[100:103], %1\n"
        "v_mfma_f32_16x16x32_f16 %2, v[84:87], v[100:103], %2\n"
        "s_waitcnt lgkmcnt(2)\n"
        "v_mfma_f32_16x16x32_f16 %3, v[88:91], v[100:103], %3\n"
        "v_mfma_f32_16x16x32_f16 %0, v[80:83], v[96:99], %0\n"
        "ds_read_b128 v[100:103], %8 offset:576\n"
        "v_mfma_f32_16x16x32_f16 %1, v[84:87], v[96:99], %1\n"
        "v_mfma_f32_16x16x32_f16 %2, v[88:91], v[96:99], %2\n"
        "v_mfma_f32_16x16x32_f16 %3, v[92:95], v[96:99], %3\n"
        "v_mfma_f32_16x16x32_f16 %3, v[224:227], v[124:127], %3\n"
        "ds_read_b128 v[96:99], %8 offset:64\n"
        "v_mfma_f32_16x16x32_f16 %2, v[224:227], v[120:123], %2\n"
        "ds_read2_b32 v[80:81], v76 offset0:198 offset1:196\n"
        "v_mfma_f32_16x16x32_f16 %3, v[228:231], v[120:123], %3\n"
        "ds_read2_b32 v[82:83], v76 offset0:194 offset1:192\n"
        "v_mfma_f32_16x16x32_f16 %1, v[224:227], v[116:119], %1\n"
        "ds_read2_b32 v[84:85], v77 offset0:198 offset1:196\n"
        "v_mfma_f32_16x16x32_f16 %2, v[228:231], v[116:119], %2\n"
        "ds_read2_b32 v[86:87], v77 offset0:194 offset1:192\n"
        "v_mfma_f32_16x16x32_f16 %3, v[232:235], v[116:119], %3\n"
        "ds_read2_b32 v[88:89], v78 offset0:198 offset1:196\n"
        "v_mfma_f32_16x16x32_f16 %0, v[224:227], v[112:115], %0\n"
        "ds_read2_b32 v[90:91], v78 offset0:194 offset1:192\n"
        "v_mfma_f32_16x16x32_f16 %1, v[228:231], v[112:115], %1\n"
        "ds_read2_b32 v[92:93], v79 offset0:198 offset1:196\n"
        "v_mfma_f32_16x16x32_f16 %2, v[232:235], v[112:115], %2\n"
        "ds_read2_b32 v[94:95], v79 offset0:194 offset1:192\n"
        "v_mfma_f32_16x16x32_f16 %3, v[236:239], v[112:115], %3\n"
        "ds_read_b128 v[124:127], %8 offset:3584\n"
        "s_waitcnt lgkmcnt(7)\n"
        "v_mfma_f32_16x16x32_f16 %0, v[228:231], v[108:111], %0\n"
        "ds_read_b128 v[120:123], %8 offset:3072\n"
        "v_mfma_f32_16x16x32_f16 %1, v[232:235], v[108:111], %1\n"
        "ds_read_b128 v[116:119], %8 offset:2560\n"
        "v_mfma_f32_16x16x32_f16 %2, v[236:239], v[108:111], %2\n"
        "ds_read_b128 v[112:115], %8 offset:2048\n"
        "v_mfma_f32_16x16x32_f16 %3, v[80:83], v[108:111], %3\n"
        "v_mfma_f32_16x16x32_f16 %0, v[232:235], v[104:107], %0\n"
        "ds_read_b128 v[108:111], %8 offset:1536\n"
        "v_mfma_f32_16x16x32_f16 %1, v[236:239], v[104:107], %1\n"
        "v_mfma_f32_16x16x32_f16 %2, v[80:83], v[104:107], %2\n"
        "s_waitcnt lgkmcnt(4)\n"
        "v_mfma_f32_16x16x32_f16 %3, v[84:87], v[104:107], %3\n"
        "v_mfma_f32_16x16x32_f16 %0, v[236:239], v[100:103], %0\n"
        "ds_read_b128 v[104:107], %8 offset:1024\n"
        "v_mfma_f32_16x16x32_f16 %1, v[80:83], v[100:103], %1\n"
        "v_mfma_f32_16x16x32_f16 %2, v[84:87], v[100:103], %2\n"
        "v_mfma_f32_16x16x32_f16 %3, v[88:91], v[100:103], %3\n"
        "v_mfma_f32_16x16x32_f16 %0, v[80:83], v[96:99], %0\n"
        "ds_read_b128 v[100:103], %8 offset:512\n"
        "v_mfma_f32_16x16x32_f16 %1, v[84:87], v[96:99], %1\n"
        "v_mfma_f32_16x16x32_f16 %2, v[88:91], v[96:99], %2\n"
        "v_mfma_f32_16x16x32_f16 %3, v[92:95], v[96:99], %3\n"
        "v_mfma_f32_16x16x32_f16 %3, v[240:243], v[124:127], %3\n"
        "ds_read_b128 v[96:99], %8 offset:0\n"
        "s_waitcnt lgkmcnt(2)\n"
        "v_mfma_f32_16x16x32_f16 %2, v[240:243], v[120:123], %2\n"
        "ds_read2_b32 v[80:81], v76 offset0:230 offset1:228\n"
        "v_mfma_f32_16x16x32_f16 %3, v[244:247], v[120:123], %3\n"
        "ds_read2_b32 v[82:83], v76 offset0:226 offset1:224\n"
        "v_mfma_f32_16x16x32_f16 %1, v[240:243], v[116:119], %1\n"
        "ds_read2_b32 v[84:85], v77 offset0:230 offset1:228\n"
        "v_mfma_f32_16x16x32_f16 %2, v[244:247], v[116:119], %2\n"
        "ds_read2_b32 v[86:87], v77 offset0:226 offset1:224\n"
        "v_mfma_f32_16x16x32_f16 %3, v[248:251], v[116:119], %3\n"
        "ds_read2_b32 v[88:89], v78 offset0:230 offset1:228\n"
        "v_mfma_f32_16x16x32_f16 %0, v[240:243], v[112:115], %0\n"
        "ds_read2_b32 v[90:91], v78 offset0:226 offset1:224\n"
        "v_mfma_f32_16x16x32_f16 %1, v[244:247], v[112:115], %1\n"
        "ds_read2_b32 v[92:93], v79 offset0:230 offset1:228\n"
        "v_mfma_f32_16x16x32_f16 %2, v[248:251], v[112:115], %2\n"
        "ds_read2_b32 v[94:95], v79 offset0:226 offset1:224\n"
        "v_mfma_f32_16x16x32_f16 %3, v[252:255], v[112:115], %3\n"
        "v_mfma_f32_16x16x32_f16 %0, v[244:247], v[108:111], %0\n"
        "v_mfma_f32_16x16x32_f16 %1, v[248:251], v[108:111], %1\n"
        "v_mfma_f32_16x16x32_f16 %2, v[252:255], v[108:111], %2\n"
        "s_waitcnt lgkmcnt(3)\n"
        "v_mfma_f32_16x16x32_f16 %3, v[80:83], v[108:111], %3\n"
        "v_mfma_f32_16x16x32_f16 %0, v[248:251], v[104:107], %0\n"
        "v_mfma_f32_16x16x32_f16 %1, v[252:255], v[104:107], %1\n"
        "v_mfma_f32_16x16x32_f16 %2, v[80:83], v[104:107], %2\n"
        "v_mfma_f32_16x16x32_f16 %3, v[84:87], v[104:107], %3\n"
        "v_mfma_f32_16x16x32_f16 %0, v[252:255], v[100:103], %0\n"
        "v_mfma_f32_16x16x32_f16 %1, v[80:83], v[100:103], %1\n"
        "v_mfma_f32_16x16x32_f16 %2, v[84:87], v[100:103], %2\n"
        "s_waitcnt lgkmcnt(0)\n"
        "v_mfma_f32_16x16x32_f16 %3, v[88:91], v[100:103], %3\n"
        "v_mfma_f32_16x16x32_f16 %0, v[80:83], v[96:99], %0\n"
        "v_mfma_f32_16x16x32_f16 %1, v[84:87], v[96:99], %1\n"
        "v_mfma_f32_16x16x32_f16 %2, v[88:91], v[96:99], %2\n"
        "v_mfma_f32_16x16x32_f16 %3, v[92:95], v[96:99], %3\n"
        "s_waitcnt vmcnt(0)\n"
        "s_nop 7\n"
        : "=&v"(acc[0]), "=&v"(acc[1]), "=&v"(acc[2]), "=&v"(acc[3]), "=&v"(wl[0]), "=&v"(wl[1]), "=&v"(wl[2])
        : "v"(pa), "v"(pb), "v"(py), "v"(ys2), "v"(cnt_addr(smem, 0)), "v"(1u), "v"(wo[0]), "v"(wo[1]), "v"(wo[2]), "s"(wsrc)
        : "memory", "s40", "s41", "scc", "v56", "v57", "v58", "v59", "v60", "v61", "v62", "v63", "v64", "v65", "v66", "v67", "v68", "v69", "v70", "v71", "v72", "v73", "v74", "v75", "v76", "v77", "v78", "v79", "v80", "v81", "v82", "v83", "v84", "v85", "v86", "v87", "v88", "v89", "v90", "v91", "v92", "v93", "v94", "v95", "v96", "v97", "v98", "v99", "v100", "v101", "v102", "v103", "v104", "v105", "v106", "v107", "v108", "v109", "v110", "v111", "v112", "v113", "v114", "v115", "v116", "v117", "v118", "v119", "v120", "v121", "v122", "v123", "v124", "v125", "v126", "v127", "v128", "v129", "v130", "v131", "v132", "v133", "v134", "v135", "v136", "v137", "v138", "v139", "v140", "v141", "v142", "v143", "v144", "v145", "v146", "v147", "v148", "v149", "v150", "v151", "v152", "v153", "v154", "v155", "v156", "v157", "v158", "v159", "v160", "v161", "v162", "v163", "v164", "v165", "v166", "v167", "v168", "v169", "v170", "v171", "v172", "v173", "v174", "v175", "v176", "v177", "v178", "v179", "v180", "v181", "v182", "v183", "v184", "v185", "v186", "v187", "v188", "v189", "v190", "v191", "v192", "v193", "v194", "v195", "v196", "v197", "v198", "v199", "v200", "v201", "v202", "v203", "v204", "v205", "v206", "v207", "v208", "v209", "v210", "v211", "v212", "v213", "v214", "v215", "v216", "v217", "v218", "v219", "v220", "v221", "v222", "v223", "v224", "v225", "v226", "v227", "v228", "v229", "v230", "v231", "v232", "v233", "v234", "v235", "v236", "v237", "v238", "v239", "v240", "v241", "v242", "v243", "v244", "v245", "v246", "v247", "v248", "v249", "v250", "v251", "v252", "v253", "v254", "v255");

            char* ys = smem + w * CV_R + (16 * r + 4 * g) * 2;
#pragma unroll
            for (int i = 0; i < 4; ++i) *(u2*)(ys + 512 * i) = u2{pk_bf(acc[i].xy * yscale), pk_bf(acc[i].zw * yscale)};
        }
        sig(smem, cY2, lane);
        STAMP(6);
        await(smem, cY2, 4);
#pragma unroll
        for (int i = 0; i < 3; ++i) *(u4*)(smem + G1_IMG1 + w * 3072 + i * 1024 + lane * 16) = wl[i];
        sig(smem, cW, lane);
    } else {
        const int t = tid & 127, grp = xcd * 20 + jj, hf = (w >> 1) & 1;
        f2 base[4][4];
#define PQ(q) ((t + 128 * (q) + 16 * ((jj * 13) >> 3)) & 511)
        const u2 dskv = *(const u2*)(dsk + grp * 4);
        BAR();
        __builtin_amdgcn_s_setprio(3);
        __builtin_amdgcn_s_sleep(XA_DELAY);
        if (hf) __builtin_amdgcn_s_sleep(XB_DELAY);
        u2 xr[4][2];
#pragma unroll
        for (int q = 0; q < 4; ++q)
#pragma unroll
            for (int e = 0; e < 2; ++e) xr[q][e] = *(const u2*)(x + (size_t)(1024 * hf + 2 * PQ(q) + e) * D + grp * 4);
        if (!hf) *(u4*)(smem + CV_U0 + (t >> 5) * CV_U + (t & 31) * 16) = u4{0, 0, 0, 0};
        u4 st[4];
        TAG();
#pragma unroll
        for (int q = 0; q < 4; ++q)
#pragma unroll
            for (int c = 0; c < 4; ++c) {
                base[q][c] = (c & 1) ? f2{bf_hi(xr[q][0][c >> 1]), bf_hi(xr[q][1][c >> 1])} : f2{bf_lo(xr[q][0][c >> 1]), bf_lo(xr[q][1][c >> 1])};
                asm volatile("" : "+v"(base[q][c]));
            }
        STAMP(7);
        await(smem, cR2, GATE);
        POLL {
            FENCE();
            bool ok = true;
#pragma unroll
            for (int q = 0; q < 4; ++q) { st[q] = ld16(wsr, rep + RP_STATS + (512 * hf + PQ(q)) * 16); ok = ok && st[q][1] == tagv && st[q][3] == tagv; }
            if (__all(ok)) break;
            __builtin_amdgcn_s_sleep(8);
        }
        STAMP(1);
        if (hf) stage_w13(w1, w3, ub0, ubs, 39 - (w & 1), 39 - BPRE, -2, lds0, lane);
        f2 uf[4][4];
        u32 up[4][4];
#pragma unroll
        for (int q = 0; q < 4; ++q) {
            const f2 m = {bf_lo(st[q][0]), bf_lo(st[q][2])};
            const f2 r = {__builtin_amdgcn_rcpf(bf_hi(st[q][0])), __builtin_amdgcn_rcpf(bf_hi(st[q][2]))};
#pragma unroll
            for (int c = 0; c < 4; ++c) uf[q][c] = f2{subS(base[q][c].x, m.x), subS(base[q][c].y, m.y)};
#pragma unroll
            for (int c = 0; c < 4; ++c) uf[q][c] = f2{mulS(uf[q][c].x, r.x), mulS(uf[q][c].y, r.y)};
#pragma unroll
            for (int c = 0; c < 4; ++c) up[q][c] = pk_h(uf[q][c].x, uf[q][c].y);
            SB();
        }
#pragma unroll
        for (int q = 0; q < 4; ++q)
#pragma unroll
            for (int c = 0; c < 4; ++c) *(u32*)(smem + CV_U0 + c * CV_U + 512 + (512 * hf + PQ(q)) * 4) = up[q][c];
        sig(smem, cU1 + hf, lane);
        __builtin_amdgcn_s_setprio(SPRIO);
        STAMP(2);
        const f2 ds01 = un_bf(dskv[0]), ds23 = un_bf(dskv[1]);
#pragma unroll
        for (int q = 0; q < 4; ++q)
#pragma unroll
            for (int c = 0; c < 4; ++c) {
                const float dc = c == 0 ? ds01.x : c == 1 ? ds01.y : c == 2 ? ds23.x : ds23.y;
                base[q][c] = f2{fmaS(uf[q][c].x, dc, base[q][c].x), fmaS(uf[q][c].y, dc, base[q][c].y)};
            }
        if (hf) {
            await(smem, cK, 4);
            stage_w13(w1, w3, ub0, ubs, 39 - BPRE - APRE - (w & 1), ASH - 1, -2, lds0, lane);
            WAITV(0);
            sig(smem, cW, lane);
            if (w == 6) {
    pollN(wsr, rep + RP_PACK, FS, 96, jj * 3, tagv, lane);
    sig(smem, cPk, lane);
}
        } else stage_w13(w1, w3, ub0, ubs, 39 - BPRE - (w & 1), 39 - BPRE - APRE, -2, lds0, lane);
        STAMP(3);
        __builtin_amdgcn_s_setprio(3);
        awaitq(smem, cY1 + hf, 4);
        STAMP(4);
        u32 y[4][4];
        const char* ysrc = smem + (hf ? 0 : CV_Y1);
        const int ystr = hf ? CV_R : 2048;
#pragma unroll
        for (int q = 0; q < 4; ++q)
#pragma unroll
            for (int c = 0; c < 4; ++c) y[q][c] = *(const u32*)(ysrc + c * ystr + PQ(q) * 4);
        SB();
#pragma unroll
        for (int q = 0; q < 4; ++q) {
            f2 hv[4];
#pragma unroll
            for (int c = 0; c < 4; ++c) hv[c] = f2{addS(base[q][c].x, bf_lo(y[q][c])), addS(base[q][c].y, bf_hi(y[q][c]))};
            st16(wsr, WS_H4 + (32 * hf + (PQ(q) >> 4)) * HS + grp * 256 + (tid & 15) * 16,
                 u4{pk_bf(hv[0].x, hv[1].x), pk_bf(hv[2].x, hv[3].x), pk_bf(hv[0].y, hv[1].y), pk_bf(hv[2].y, hv[3].y)});
        }
        STAMP(5);
        WAITV(0);
        if (sig_ret(smem, cD1A + hf, lane) == 1u && lane < 8) st4(wsr, WS_REP + lane * REPS + RP_FLAG1 + hf * F1H + grp * FS, tagv);
        STAMP(6);
        if (!hf) {
            await(smem, cD1A, 2);
            stage_w13(w1, w3, ub0, ubs, 4 + (w & 1), ASH, 2, lds0, lane);
            if (TOUCHA && w == 5) touch_h(wsr, rep + RP_FLAG1, WS_H4 + (xcd & 4) * 4 * HS, jj, tagv, lane);
            WAITV(0);
            sig(smem, cW, lane);
        }
    }

    asm volatile("" :: "v"(pfc));
    const int th = w >> 2;
    if (th) __builtin_amdgcn_s_setprio(3);
    const int stok = mb * 128 + n1 * 4;
    if (w == 4) { pollN(wsr, rep + RP_FLAG1, FS, 160, jj * 5, tagv, lane); sig(smem, fA, lane); }
    {
        const int kq = w & 3;
        auto rest = [&](auto TH_) {
            int lane = lane_id();
            constexpr int TH = decltype(TH_)::value, NSW = TH ? 5 : 7;
            const int s0 = kq * 12 + (TH ? 7 : 0);
            const int woff = WS_P2 + nt * P2S + s0 * 5120 + lane * 16;
            u4 wf[NSW * 5];
            if (TH) {
                if (w == 5) {
                    await(smem, cPk, 1);
        const int l = min(lane, 47), mat = l / 24, hb = (l % 24) / 12;
        u4 pv[20];
#pragma unroll
        for (int i = 0; i < 20; ++i) pv[i] = ld16(wsr, WS_CS + ((mat * 40 + hb * 20) * F + (ub0 + ((l % 12) >> 2) * ubs) * 16 + (l & 3) * 4) * 4, i * F * 4);
        SB();
        f4 c = {};
#pragma unroll
        for (int i = 0; i < 20; ++i)
#pragma unroll
            for (int j = 0; j < 4; ++j) c[j] = addS(c[j], __uint_as_float(pv[i][j]));
        await(smem, cW, 8);
        f4* part = (f4*)(smem + G1_CS + 512);
        part[l] = c;
        if (lane < 24) {
            const f4 a = part[(lane / 12) * 24 + lane % 12], b = part[(lane / 12) * 24 + 12 + lane % 12];
            ((f4*)(smem + G1_CS))[lane] = f4{addS(a[0], b[0]), addS(a[1], b[1]), addS(a[2], b[2]), addS(a[3], b[3])};
        }
                    sig(smem, cCS, lane);
                }
                if (w == 5) {
                    await(smem, fA, 1);
                    u4 sv[5];
                    ln2_load(sv, wsr, stok, lane);
                    ln2_put(wsr, stok, ln2_calc(sv, lane), tagv, lane);
                }
                if (POLLB_FIRST) {
                if (w == POLLB_FIRST) {
                    pollN(wsr, rep + RP_FLAG1 + F1H, FS, 160, jj * 5, tagv, lane); sig(smem, fB, lane);
                    if (blockIdx.x == 0) { await(smem, cPk, 1); if (lane == 0) st4(wsr, WS_EPOCH, tagv); }
                }
                }
                if (LP0) {
                    await(smem, cPk, 1);
                    __builtin_amdgcn_s_setprio(LPRIO);
#pragma unroll
                    for (int f = 0; f < LP0; ++f) { wf[f] = ld16(wsr, woff + (f & 3) * 1024, (f >> 2) * 4096); PACE0(); SB(); }
                    __builtin_amdgcn_s_setprio(3);
                }
                if (!POLLB_FIRST) {
                if (w == 4) {
                    pollN(wsr, rep + RP_FLAG1 + F1H, FS, 160, jj * 5, tagv, lane); sig(smem, fB, lane);
                    if (blockIdx.x == 0) { await(smem, cPk, 1); if (lane == 0) st4(wsr, WS_EPOCH, tagv); }
                }
                }
                if (TOUCHB && w == 6) touch_h(wsr, rep + RP_FLAG1 + F1H, WS_H4 + (32 + (xcd & 4) * 4) * HS, jj, tagv, lane);
                if (w == 7) {
                    await(smem, fB, 1);
                    u4 sv[5];
                    ln2_load(sv, wsr, stok + 1024, lane);
                    ln2_put(wsr, stok + 1024, ln2_calc(sv, lane), tagv, lane);
                }
            } else if (NE0) {
                await(smem, cPk, 1);
#pragma unroll
                for (int f = 0; f < NE0; ++f) { wf[f] = ld16(wsr, woff + (f & 3) * 1024, (f >> 2) * 4096); SB(); }
            }
            await(smem, fA + TH, 1);
            await(smem, cW, 8);
            STAMP(8);
            const int tb = TH * 32 + mb * 4 + kq;
            __builtin_amdgcn_s_setprio(TH ? 1 : 2);
            f16v acc[3];
            u2 st2;
            const int tok = tb * 32 + 2 * (lane & 15) + ((lane >> 4) & 1);
            {
                const u32 fb = lds0 + G1_IMG1 + ((lane >> 4) & 1) * 61568 + (8 * (lane >> 5) + ((lane & 15) >> 2)) * 96 + (((lane & 1) << 1) | ((lane & 2) >> 1)) * 8;
                const u32 hoff = WS_H4 + tb * HS + lane * 16, soff = WS_STAT2 + tok * 8;
                if constexpr (TH) {
    asm volatile(
        "s_mov_b32 s40, 0\n"
        "buffer_load_dwordx4 v[208:211], %4, %7, s40 offen offset:0 sc1\n"
        "buffer_load_dwordx4 v[212:215], %4, %7, s40 offen offset:1024 sc1\n"
        "buffer_load_dwordx4 v[216:219], %4, %7, s40 offen offset:2048 sc1\n"
        "buffer_load_dwordx4 v[220:223], %4, %7, s40 offen offset:3072 sc1\n"
        "s_mov_b32 s40, 4096\n"
        "buffer_load_dwordx4 v[224:227], %4, %7, s40 offen offset:0 sc1\n"
        "buffer_load_dwordx4 v[228:231], %4, %7, s40 offen offset:1024 sc1\n"
        "buffer_load_dwordx4 v[232:235], %4, %7, s40 offen offset:2048 sc1\n"
        "buffer_load_dwordx4 v[236:239], %4, %7, s40 offen offset:3072 sc1\n"
        "ds_read_b64_tr_b16 v[184:185], %5 offset:0\n"
        "ds_read_b64_tr_b16 v[186:187], %5 offset:384\n"
        "ds_read_b64_tr_b16 v[188:189], %5 offset:32\n"
        "ds_read_b64_tr_b16 v[190:191], %5 offset:416\n"
        "ds_read_b64_tr_b16 v[192:193], %5 offset:64\n"
        "ds_read_b64_tr_b16 v[194:195], %5 offset:448\n"
        "ds_read_b64_tr_b16 v[196:197], %5 offset:1536\n"
        "ds_read_b64_tr_b16 v[198:199], %5 offset:1920\n"
        "ds_read_b64_tr_b16 v[200:201], %5 offset:1568\n"
        "ds_read_b64_tr_b16 v[202:203], %5 offset:1952\n"
        "s_waitcnt vmcnt(7)\n"
        "v_permlane16_swap_b32 v208, v210\n"
        "v_permlane16_swap_b32 v209, v211\n"
        "s_nop 0\n"
        "s_waitcnt lgkmcnt(8)\n"
        "v_mfma_f32_32x32x16_bf16 %0, v[184:187], v[208:211], 0\n"
        "ds_read_b64_tr_b16 v[204:205], %5 offset:1600\n"
        "ds_read_b64_tr_b16 v[206:207], %5 offset:1984\n"
        "s_mov_b32 s40, 8192\n"
        "buffer_load_dwordx4 v[240:243], %4, %7, s40 offen offset:0 sc1\n"
        "s_waitcnt lgkmcnt(8)\n"
        "v_mfma_f32_32x32x16_bf16 %1, v[188:191], v[208:211], 0\n"
        "ds_read_b64_tr_b16 v[184:185], %5 offset:3072\n"
        "ds_read_b64_tr_b16 v[186:187], %5 offset:3456\n"
        "s_waitcnt lgkmcnt(8)\n"
        "v_mfma_f32_32x32x16_bf16 %2, v[192:195], v[208:211], 0\n"
        "s_waitcnt vmcnt(7)\n"
        "v_permlane16_swap_b32 v212, v214\n"
        "v_permlane16_swap_b32 v213, v215\n"
        "ds_read_b64_tr_b16 v[188:189], %5 offset:3104\n"
        "ds_read_b64_tr_b16 v[190:191], %5 offset:3488\n"
        "s_waitcnt lgkmcnt(8)\n"
        "v_mfma_f32_32x32x16_bf16 %0, v[196:199], v[212:215], %0\n"
        "ds_read_b64_tr_b16 v[192:193], %5 offset:3136\n"
        "ds_read_b64_tr_b16 v[194:195], %5 offset:3520\n"
        "buffer_load_dwordx4 v[244:247], %4, %7, s40 offen offset:1024 sc1\n"
        "s_waitcnt lgkmcnt(8)\n"
        "v_mfma_f32_32x32x16_bf16 %1, v[200:203], v[212:215], %1\n"
        "ds_read_b64_tr_b16 v[196:197], %5 offset:4608\n"
        "ds_read_b64_tr_b16 v[198:199], %5 offset:4992\n"
        "s_waitcnt lgkmcnt(8)\n"
        "v_mfma_f32_32x32x16_bf16 %2, v[204:207], v[212:215], %2\n"
        "s_waitcnt vmcnt(7)\n"
        "v_permlane16_swap_b32 v216, v218\n"
        "v_permlane16_swap_b32 v217, v219\n"
        "ds_read_b64_tr_b16 v[200:201], %5 offset:4640\n"
        "ds_read_b64_tr_b16 v[202:203], %5 offset:5024\n"
        "s_waitcnt lgkmcnt(8)\n"
        "v_mfma_f32_32x32x16_bf16 %0, v[184:187], v[216:219], %0\n"
        "ds_read_b64_tr_b16 v[204:205], %5 offset:4672\n"
        "ds_read_b64_tr_b16 v[206:207], %5 offset:5056\n"
        "buffer_load_dwordx4 v[248:251], %4, %7, s40 offen offset:2048 sc1\n"
        "s_waitcnt lgkmcnt(8)\n"
        "v_mfma_f32_32x32x16_bf16 %1, v[188:191], v[216:219], %1\n"
        "ds_read_b64_tr_b16 v[184:185], %5 offset:6144\n"
        "ds_read_b64_tr_b16 v[186:187], %5 offset:6528\n"
        "s_waitcnt lgkmcnt(8)\n"
        "v_mfma_f32_32x32x16_bf16 %2, v[192:195], v[216:219], %2\n"
        "s_waitcnt vmcnt(7)\n"
        "v_permlane16_swap_b32 v220, v222\n"
        "v_permlane16_swap_b32 v221, v223\n"
        "ds_read_b64_tr_b16 v[188:189], %5 offset:6176\n"
        "ds_read_b64_tr_b16 v[190:191], %5 offset:6560\n"
        "s_waitcnt lgkmcnt(8)\n"
        "v_mfma_f32_32x32x16_bf16 %0, v[196:199], v[220:223], %0\n"
        "ds_read_b64_tr_b16 v[192:193], %5 offset:6208\n"
        "ds_read_b64_tr_b16 v[194:195], %5 offset:6592\n"
        "buffer_load_dwordx4 v[252:255], %4, %7, s40 offen offset:3072 sc1\n"
        "s_waitcnt lgkmcnt(8)\n"
        "v_mfma_f32_32x32x16_bf16 %1, v[200:203], v[220:223], %1\n"
        "ds_read_b64_tr_b16 v[196:197], %5 offset:7680\n"
        "ds_read_b64_tr_b16 v[198:199], %5 offset:8064\n"
        "s_waitcnt lgkmcnt(8)\n"
        "v_mfma_f32_32x32x16_bf16 %2, v[204:207], v[220:223], %2\n"
        "s_waitcnt vmcnt(7)\n"
        "v_permlane16_swap_b32 v224, v226\n"
        "v_permlane16_swap_b32 v225, v227\n"
        "ds_read_b64_tr_b16 v[200:201], %5 offset:7712\n"
        "ds_read_b64_tr_b16 v[202:203], %5 offset:8096\n"
        "s_waitcnt lgkmcnt(8)\n"
        "v_mfma_f32_32x32x16_bf16 %0, v[184:187], v[224:227], %0\n"
        "ds_read_b64_tr_b16 v[204:205], %5 offset:7744\n"
        "ds_read_b64_tr_b16 v[206:207], %5 offset:8128\n"
        "s_mov_b32 s40, 12288\n"
        "buffer_load_dwordx4 v[208:211], %4, %7, s40 offen offset:0 sc1\n"
        "s_waitcnt lgkmcnt(8)\n"
        "v_mfma_f32_32x32x16_bf16 %1, v[188:191], v[224:227], %1\n"
        "ds_read_b64_tr_b16 v[184:185], %5 offset:9216\n"
        "ds_read_b64_tr_b16 v[186:187], %5 offset:9600\n"
        "s_waitcnt lgkmcnt(8)\n"
        "v_mfma_f32_32x32x16_bf16 %2, v[192:195], v[224:227], %2\n"
        "s_waitcnt vmcnt(7)\n"
        "v_permlane16_swap_b32 v228, v230\n"
        "v_permlane16_swap_b32 v229, v231\n"
        "ds_read_b64_tr_b16 v[188:189], %5 offset:9248\n"
        "ds_read_b64_tr_b16 v[190:191], %5 offset:9632\n"
        "s_waitcnt lgkmcnt(8)\n"
        "v_mfma_f32_32x32x16_bf16 %0, v[196:199], v[228:231], %0\n"
        "ds_read_b64_tr_b16 v[192:193], %5 offset:9280\n"
        "ds_read_b64_tr_b16 v[194:195], %5 offset:9664\n"
        "buffer_load_dwordx4 v[212:215], %4, %7, s40 offen offset:1024 sc1\n"
        "s_waitcnt lgkmcnt(8)\n"
        "v_mfma_f32_32x32x16_bf16 %1, v[200:203], v[228:231], %1\n"
        "ds_read_b64_tr_b16 v[196:197], %5 offset:10752\n"
        "ds_read_b64_tr_b16 v[198:199], %5 offset:11136\n"
        "s_waitcnt lgkmcnt(8)\n"
        "v_mfma_f32_32x32x16_bf16 %2, v[204:207], v[228:231], %2\n"
        "s_waitcnt vmcnt(7)\n"
        "v_permlane16_swap_b32 v232, v234\n"
        "v_permlane16_swap_b32 v233, v235\n"
        "ds_read_b64_tr_b16 v[200:201], %5 offset:10784\n"
        "ds_read_b64_tr_b16 v[202:203], %5 offset:11168\n"
        "s_waitcnt lgkmcnt(8)\n"
        "v_mfma_f32_32x32x16_bf16 %0, v[184:187], v[232:235], %0\n"
        "ds_read_b64_tr_b16 v[204:205], %5 offset:10816\n"
        "ds_read_b64_tr_b16 v[206:207], %5 offset:11200\n"
        "buffer_load_dwordx4 v[216:219], %4, %7, s40 offen offset:2048 sc1\n"
        "s_waitcnt lgkmcnt(8)\n"
        "v_mfma_f32_32x32x16_bf16 %1, v[188:191], v[232:235], %1\n"
        "ds_read_b64_tr_b16 v[184:185], %5 offset:12288\n"
        "ds_read_b64_tr_b16 v[186:187], %5 offset:12672\n"
        "s_waitcnt lgkmcnt(8)\n"
        "v_mfma_f32_32x32x16_bf16 %2, v[192:195], v[232:235], %2\n"
        "s_waitcnt vmcnt(7)\n"
        "v_permlane16_swap_b32 v236, v238\n"
        "v_permlane16_swap_b32 v237, v239\n"
        "ds_read_b64_tr_b16 v[188:189], %5 offset:12320\n"
        "ds_read_b64_tr_b16 v[190:191], %5 offset:12704\n"
        "s_waitcnt lgkmcnt(8)\n"
        "v_mfma_f32_32x32x16_bf16 %0, v[196:199], v[236:239], %0\n"
        "ds_read_b64_tr_b16 v[192:193], %5 offset:12352\n"
        "ds_read_b64_tr_b16 v[194:195], %5 offset:12736\n"
        "buffer_load_dwordx4 v[220:223], %4, %7, s40 offen offset:3072 sc1\n"
        "s_waitcnt lgkmcnt(8)\n"
        "v_mfma_f32_32x32x16_bf16 %1, v[200:203], v[236:239], %1\n"
        "ds_read_b64_tr_b16 v[196:197], %5 offset:13824\n"
        "ds_read_b64_tr_b16 v[198:199], %5 offset:14208\n"
        "s_waitcnt lgkmcnt(8)\n"
        "v_mfma_f32_32x32x16_bf16 %2, v[204:207], v[236:239], %2\n"
        "s_waitcnt vmcnt(7)\n"
        "v_permlane16_swap_b32 v240, v242\n"
        "v_permlane16_swap_b32 v241, v243\n"
        "ds_read_b64_tr_b16 v[200:201], %5 offset:13856\n"
        "ds_read_b64_tr_b16 v[202:203], %5 offset:14240\n"
        "s_waitcnt lgkmcnt(8)\n"
        "v_mfma_f32_32x32x16_bf16 %0, v[184:187], v[240:243], %0\n"
        "ds_read_b64_tr_b16 v[204:205], %5 offset:13888\n"
        "ds_read_b64_tr_b16 v[206:207], %5 offset:14272\n"
        "s_mov_b32 s40, 16384\n"
        "buffer_load_dwordx4 v[224:227], %4, %7, s40 offen offset:0 sc1\n"
        "s_waitcnt lgkmcnt(8)\n"
        "v_mfma_f32_32x32x16_bf16 %1, v[188:191], v[240:243], %1\n"
        "ds_read_b64_tr_b16 v[184:185], %5 offset:15360\n"
        "ds_read_b64_tr_b16 v[186:187], %5 offset:15744\n"
        "s_waitcnt lgkmcnt(8)\n"
        "v_mfma_f32_32x32x16_bf16 %2, v[192:195], v[240:243], %2\n"
        "s_waitcnt vmcnt(7)\n"
        "v_permlane16_swap_b32 v244, v246\n"
        "v_permlane16_swap_b32 v245, v247\n"
        "ds_read_b64_tr_b16 v[188:189], %5 offset:15392\n"
        "ds_read_b64_tr_b16 v[190:191], %5 offset:15776\n"
        "s_waitcnt lgkmcnt(8)\n"
        "v_mfma_f32_32x32x16_bf16 %0, v[196:199], v[244:247], %0\n"
        "ds_read_b64_tr_b16 v[192:193], %5 offset:15424\n"
        "ds_read_b64_tr_b16 v[194:195], %5 offset:15808\n"
        "buffer_load_dwordx4 v[228:231], %4, %7, s40 offen offset:1024 sc1\n"
        "s_waitcnt lgkmcnt(8)\n"
        "v_mfma_f32_32x32x16_bf16 %1, v[200:203], v[244:247], %1\n"
        "ds_read_b64_tr_b16 v[196:197], %5 offset:16896\n"
        "ds_read_b64_tr_b16 v[198:199], %5 offset:17280\n"
        "s_waitcnt lgkmcnt(8)\n"
        "v_mfma_f32_32x32x16_bf16 %2, v[204:207], v[244:247], %2\n"
        "s_waitcnt vmcnt(7)\n"
        "v_permlane16_swap_b32 v248, v250\n"
        "v_permlane16_swap_b32 v249, v251\n"
        "ds_read_b64_tr_b16 v[200:201], %5 offset:16928\n"
        "ds_read_b64_tr_b16 v[202:203], %5 offset:17312\n"
        "s_waitcnt lgkmcnt(8)\n"
        "v_mfma_f32_32x32x16_bf16 %0, v[184:187], v[248:251], %0\n"
        "ds_read_b64_tr_b16 v[204:205], %5 offset:16960\n"
        "ds_read_b64_tr_b16 v[206:207], %5 offset:17344\n"
        "buffer_load_dwordx4 v[232:235], %4, %7, s40 offen offset:2048 sc1\n"
        "s_waitcnt lgkmcnt(8)\n"
        "v_mfma_f32_32x32x16_bf16 %1, v[188:191], v[248:251], %1\n"
        "ds_read_b64_tr_b16 v[184:185], %5 offset:18432\n"
        "ds_read_b64_tr_b16 v[186:187], %5 offset:18816\n"
        "s_waitcnt lgkmcnt(8)\n"
        "v_mfma_f32_32x32x16_bf16 %2, v[192:195], v[248:251], %2\n"
        "s_waitcnt vmcnt(7)\n"
        "v_permlane16_swap_b32 v252, v254\n"
        "v_permlane16_swap_b32 v253, v255\n"
        "ds_read_b64_tr_b16 v[188:189], %5 offset:18464\n"
        "ds_read_b64_tr_b16 v[190:191], %5 offset:18848\n"
        "s_waitcnt lgkmcnt(8)\n"
        "v_mfma_f32_32x32x16_bf16 %0, v[196:199], v[252:255], %0\n"
        "ds_read_b64_tr_b16 v[192:193], %5 offset:18496\n"
        "ds_read_b64_tr_b16 v[194:195], %5 offset:18880\n"
        "buffer_load_dwordx4 v[236:239], %4, %7, s40 offen offset:3072 sc1\n"
        "s_waitcnt lgkmcnt(8)\n"
        "v_mfma_f32_32x32x16_bf16 %1, v[200:203], v[252:255], %1\n"
        "ds_read_b64_tr_b16 v[196:197], %5 offset:19968\n"
        "ds_read_b64_tr_b16 v[198:199], %5 offset:20352\n"
        "s_waitcnt lgkmcnt(8)\n"
        "v_mfma_f32_32x32x16_bf16 %2, v[204:207], v[252:255], %2\n"
        "s_waitcnt vmcnt(7)\n"
        "v_permlane16_swap_b32 v208, v210\n"
        "v_permlane16_swap_b32 v209, v211\n"
        "ds_read_b64_tr_b16 v[200:201], %5 offset:20000\n"
        "ds_read_b64_tr_b16 v[202:203], %5 offset:20384\n"
        "s_waitcnt lgkmcnt(8)\n"
        "v_mfma_f32_32x32x16_bf16 %0, v[184:187], v[208:211], %0\n"
        "ds_read_b64_tr_b16 v[204:205], %5 offset:20032\n"
        "ds_read_b64_tr_b16 v[206:207], %5 offset:20416\n"
        "s_mov_b32 s40, 20480\n"
        "buffer_load_dwordx4 v[240:243], %4, %7, s40 offen offset:0 sc1\n"
        "s_waitcnt lgkmcnt(8)\n"
        "v_mfma_f32_32x32x16_bf16 %1, v[188:191], v[208:211], %1\n"
        "ds_read_b64_tr_b16 v[184:185], %5 offset:21504\n"
        "ds_read_b64_tr_b16 v[186:187], %5 offset:21888\n"
        "s_waitcnt lgkmcnt(8)\n"
        "v_mfma_f32_32x32x16_bf16 %2, v[192:195], v[208:211], %2\n"
        "s_waitcnt vmcnt(7)\n"
        "v_permlane16_swap_b32 v212, v214\n"
        "v_permlane16_swap_b32 v213, v215\n"
        "ds_read_b64_tr_b16 v[188:189], %5 offset:21536\n"
        "ds_read_b64_tr_b16 v[190:191], %5 offset:21920\n"
        "s_waitcnt lgkmcnt(8)\n"
        "v_mfma_f32_32x32x16_bf16 %0, v[196:199], v[212:215], %0\n"
        "ds_read_b64_tr_b16 v[192:193], %5 offset:21568\n"
        "ds_read_b64_tr_b16 v[194:195], %5 offset:21952\n"
        "buffer_load_dwordx4 v[244:247], %4, %7, s40 offen offset:1024 sc1\n"
        "s_waitcnt lgkmcnt(8)\n"
        "v_mfma_f32_32x32x16_bf16 %1, v[200:203], v[212:215], %1\n"
        "ds_read_b64_tr_b16 v[196:197], %5 offset:23040\n"
        "ds_read_b64_tr_b16 v[198:199], %5 offset:23424\n"
        "s_waitcnt lgkmcnt(8)\n"
        "v_mfma_f32_32x32x16_bf16 %2, v[204:207], v[212:215], %2\n"
        "s_waitcnt vmcnt(7)\n"
        "v_permlane16_swap_b32 v216, v218\n"
        "v_permlane16_swap_b32 v217, v219\n"
        "ds_read_b64_tr_b16 v[200:201], %5 offset:23072\n"
        "ds_read_b64_tr_b16 v[202:203], %5 offset:23456\n"
        "s_waitcnt lgkmcnt(8)\n"
        "v_mfma_f32_32x32x16_bf16 %0, v[184:187], v[216:219], %0\n"
        "ds_read_b64_tr_b16 v[204:205], %5 offset:23104\n"
        "ds_read_b64_tr_b16 v[206:207], %5 offset:23488\n"
        "buffer_load_dwordx4 v[248:251], %4, %7, s40 offen offset:2048 sc1\n"
        "s_waitcnt lgkmcnt(8)\n"
        "v_mfma_f32_32x32x16_bf16 %1, v[188:191], v[216:219], %1\n"
        "ds_read_b64_tr_b16 v[184:185], %5 offset:24576\n"
        "ds_read_b64_tr_b16 v[186:187], %5 offset:24960\n"
        "s_waitcnt lgkmcnt(8)\n"
        "v_mfma_f32_32x32x16_bf16 %2, v[192:195], v[216:219], %2\n"
        "s_waitcnt vmcnt(7)\n"
        "v_permlane16_swap_b32 v220, v222\n"
        "v_permlane16_swap_b32 v221, v223\n"
        "ds_read_b64_tr_b16 v[188:189], %5 offset:24608\n"
        "ds_read_b64_tr_b16 v[190:191], %5 offset:24992\n"
        "s_waitcnt lgkmcnt(8)\n"
        "v_mfma_f32_32x32x16_bf16 %0, v[196:199], v[220:223], %0\n"
        "ds_read_b64_tr_b16 v[192:193], %5 offset:24640\n"
        "ds_read_b64_tr_b16 v[194:195], %5 offset:25024\n"
        "buffer_load_dwordx4 v[252:255], %4, %7, s40 offen offset:3072 sc1\n"
        "s_waitcnt lgkmcnt(8)\n"
        "v_mfma_f32_32x32x16_bf16 %1, v[200:203], v[220:223], %1\n"
        "ds_read_b64_tr_b16 v[196:197], %5 offset:26112\n"
        "ds_read_b64_tr_b16 v[198:199], %5 offset:26496\n"
        "s_waitcnt lgkmcnt(8)\n"
        "v_mfma_f32_32x32x16_bf16 %2, v[204:207], v[220:223], %2\n"
        "s_waitcnt vmcnt(7)\n"
        "v_permlane16_swap_b32 v224, v226\n"
        "v_permlane16_swap_b32 v225, v227\n"
        "ds_read_b64_tr_b16 v[200:201], %5 offset:26144\n"
        "ds_read_b64_tr_b16 v[202:203], %5 offset:26528\n"
        "s_waitcnt lgkmcnt(8)\n"
        "v_mfma_f32_32x32x16_bf16 %0, v[184:187], v[224:227], %0\n"
        "ds_read_b64_tr_b16 v[204:205], %5 offset:26176\n"
        "ds_read_b64_tr_b16 v[206:207], %5 offset:26560\n"
        "s_mov_b32 s40, 24576\n"
        "buffer_load_dwordx4 v[208:211], %4, %7, s40 offen offset:0 sc1\n"
        "s_waitcnt lgkmcnt(8)\n"
        "v_mfma_f32_32x32x16_bf16 %1, v[188:191], v[224:227], %1\n"
        "ds_read_b64_tr_b16 v[184:185], %5 offset:27648\n"
        "ds_read_b64_tr_b16 v[186:187], %5 offset:28032\n"
        "s_waitcnt lgkmcnt(8)\n"
        "v_mfma_f32_32x32x16_bf16 %2, v[192:195], v[224:227], %2\n"
        "s_waitcnt vmcnt(7)\n"
        "v_permlane16_swap_b32 v228, v230\n"
        "v_permlane16_swap_b32 v229, v231\n"
        "ds_read_b64_tr_b16 v[188:189], %5 offset:27680\n"
        "ds_read_b64_tr_b16 v[190:191], %5 offset:28064\n"
        "s_waitcnt lgkmcnt(8)\n"
        "v_mfma_f32_32x32x16_bf16 %0, v[196:199], v[228:231], %0\n"
        "ds_read_b64_tr_b16 v[192:193], %5 offset:27712\n"
        "ds_read_b64_tr_b16 v[194:195], %5 offset:28096\n"
        "buffer_load_dwordx4 v[212:215], %4, %7, s40 offen offset:1024 sc1\n"
        "s_waitcnt lgkmcnt(8)\n"
        "v_mfma_f32_32x32x16_bf16 %1, v[200:203], v[228:231], %1\n"
        "ds_read_b64_tr_b16 v[196:197], %5 offset:29184\n"
        "ds_read_b64_tr_b16 v[198:199], %5 offset:29568\n"
        "s_waitcnt lgkmcnt(8)\n"
        "v_mfma_f32_32x32x16_bf16 %2, v[204:207], v[228:231], %2\n"
        "s_waitcnt vmcnt(7)\n"
        "v_permlane16_swap_b32 v232, v234\n"
        "v_permlane16_swap_b32 v233, v235\n"
        "ds_read_b64_tr_b16 v[200:201], %5 offset:29216\n"
        "ds_read_b64_tr_b16 v[202:203], %5 offset:29600\n"
        "s_waitcnt lgkmcnt(8)\n"
        "v_mfma_f32_32x32x16_bf16 %0, v[184:187], v[232:235], %0\n"
        "ds_read_b64_tr_b16 v[204:205], %5 offset:29248\n"
        "ds_read_b64_tr_b16 v[206:207], %5 offset:29632\n"
        "buffer_load_dwordx4 v[216:219], %4, %7, s40 offen offset:2048 sc1\n"
        "s_waitcnt lgkmcnt(8)\n"
        "v_mfma_f32_32x32x16_bf16 %1, v[188:191], v[232:235], %1\n"
        "ds_read_b64_tr_b16 v[184:185], %5 offset:30720\n"
        "ds_read_b64_tr_b16 v[186:187], %5 offset:31104\n"
        "s_waitcnt lgkmcnt(8)\n"
        "v_mfma_f32_32x32x16_bf16 %2, v[192:195], v[232:235], %2\n"
        "s_waitcnt vmcnt(7)\n"
        "v_permlane16_swap_b32 v236, v238\n"
        "v_permlane16_swap_b32 v237, v239\n"
        "ds_read_b64_tr_b16 v[188:189], %5 offset:30752\n"
        "ds_read_b64_tr_b16 v[190:191], %5 offset:31136\n"
        "s_waitcnt lgkmcnt(8)\n"
        "v_mfma_f32_32x32x16_bf16 %0, v[196:199], v[236:239], %0\n"
        "ds_read_b64_tr_b16 v[192:193], %5 offset:30784\n"
        "ds_read_b64_tr_b16 v[194:195], %5 offset:31168\n"
        "buffer_load_dwordx4 v[220:223], %4, %7, s40 offen offset:3072 sc1\n"
        "s_waitcnt lgkmcnt(8)\n"
        "v_mfma_f32_32x32x16_bf16 %1, v[200:203], v[236:239], %1\n"
        "ds_read_b64_tr_b16 v[196:197], %5 offset:32256\n"
        "ds_read_b64_tr_b16 v[198:199], %5 offset:32640\n"
        "s_waitcnt lgkmcnt(8)\n"
        "v_mfma_f32_32x32x16_bf16 %2, v[204:207], v[236:239], %2\n"
        "s_waitcnt vmcnt(7)\n"
        "v_permlane16_swap_b32 v240, v242\n"
        "v_permlane16_swap_b32 v241, v243\n"
        "ds_read_b64_tr_b16 v[200:201], %5 offset:32288\n"
        "ds_read_b64_tr_b16 v[202:203], %5 offset:32672\n"
        "s_waitcnt lgkmcnt(8)\n"
        "v_mfma_f32_32x32x16_bf16 %0, v[184:187], v[240:243], %0\n"
        "ds_read_b64_tr_b16 v[204:205], %5 offset:32320\n"
        "ds_read_b64_tr_b16 v[206:207], %5 offset:32704\n"
        "s_mov_b32 s40, 28672\n"
        "buffer_load_dwordx4 v[224:227], %4, %7, s40 offen offset:0 sc1\n"
        "s_waitcnt lgkmcnt(8)\n"
        "v_mfma_f32_32x32x16_bf16 %1, v[188:191], v[240:243], %1\n"
        "ds_read_b64_tr_b16 v[184:185], %5 offset:33792\n"
        "ds_read_b64_tr_b16 v[186:187], %5 offset:34176\n"
        "s_waitcnt lgkmcnt(8)\n"
        "v_mfma_f32_32x32x16_bf16 %2, v[192:195], v[240:243], %2\n"
        "s_waitcnt vmcnt(7)\n"
        "v_permlane16_swap_b32 v244, v246\n"
        "v_permlane16_swap_b32 v245, v247\n"
        "ds_read_b64_tr_b16 v[188:189], %5 offset:33824\n"
        "ds_read_b64_tr_b16 v[190:191], %5 offset:34208\n"
        "s_waitcnt lgkmcnt(8)\n"
        "v_mfma_f32_32x32x16_bf16 %0, v[196:199], v[244:247], %0\n"
        "ds_read_b64_tr_b16 v[192:193], %5 offset:33856\n"
        "ds_read_b64_tr_b16 v[194:195], %5 offset:34240\n"
        "buffer_load_dwordx4 v[228:231], %4, %7, s40 offen offset:1024 sc1\n"
        "s_waitcnt lgkmcnt(8)\n"
        "v_mfma_f32_32x32x16_bf16 %1, v[200:203], v[244:247], %1\n"
        "ds_read_b64_tr_b16 v[196:197], %5 offset:35328\n"
        "ds_read_b64_tr_b16 v[198:199], %5 offset:35712\n"
        "s_waitcnt lgkmcnt(8)\n"
        "v_mfma_f32_32x32x16_bf16 %2, v[204:207], v[244:247], %2\n"
        "s_waitcnt vmcnt(7)\n"
        "v_permlane16_swap_b32 v248, v250\n"
        "v_permlane16_swap_b32 v249, v251\n"
        "ds_read_b64_tr_b16 v[200:201], %5 offset:35360\n"
        "ds_read_b64_tr_b16 v[202:203], %5 offset:35744\n"
        "s_waitcnt lgkmcnt(8)\n"
        "v_mfma_f32_32x32x16_bf16 %0, v[184:187], v[248:251], %0\n"
        "ds_read_b64_tr_b16 v[204:205], %5 offset:35392\n"
        "ds_read_b64_tr_b16 v[206:207], %5 offset:35776\n"
        "buffer_load_dwordx4 v[232:235], %4, %7, s40 offen offset:2048 sc1\n"
        "s_waitcnt lgkmcnt(8)\n"
        "v_mfma_f32_32x32x16_bf16 %1, v[188:191], v[248:251], %1\n"
        "ds_read_b64_tr_b16 v[184:185], %5 offset:36864\n"
        "ds_read_b64_tr_b16 v[186:187], %5 offset:37248\n"
        "s_waitcnt lgkmcnt(8)\n"
        "v_mfma_f32_32x32x16_bf16 %2, v[192:195], v[248:251], %2\n"
        "s_waitcnt vmcnt(7)\n"
        "v_permlane16_swap_b32 v252, v254\n"
        "v_permlane16_swap_b32 v253, v255\n"
        "ds_read_b64_tr_b16 v[188:189], %5 offset:36896\n"
        "ds_read_b64_tr_b16 v[190:191], %5 offset:37280\n"
        "s_waitcnt lgkmcnt(8)\n"
        "v_mfma_f32_32x32x16_bf16 %0, v[196:199], v[252:255], %0\n"
        "ds_read_b64_tr_b16 v[192:193], %5 offset:36928\n"
        "ds_read_b64_tr_b16 v[194:195], %5 offset:37312\n"
        "buffer_load_dwordx4 v[236:239], %4, %7, s40 offen offset:3072 sc1\n"
        "s_waitcnt lgkmcnt(8)\n"
        "v_mfma_f32_32x32x16_bf16 %1, v[200:203], v[252:255], %1\n"
        "ds_read_b64_tr_b16 v[196:197], %5 offset:38400\n"
        "ds_read_b64_tr_b16 v[198:199], %5 offset:38784\n"
        "s_waitcnt lgkmcnt(8)\n"
        "v_mfma_f32_32x32x16_bf16 %2, v[204:207], v[252:255], %2\n"
        "s_waitcnt vmcnt(7)\n"
        "v_permlane16_swap_b32 v208, v210\n"
        "v_permlane16_swap_b32 v209, v211\n"
        "ds_read_b64_tr_b16 v[200:201], %5 offset:38432\n"
        "ds_read_b64_tr_b16 v[202:203], %5 offset:38816\n"
        "s_waitcnt lgkmcnt(8)\n"
        "v_mfma_f32_32x32x16_bf16 %0, v[184:187], v[208:211], %0\n"
        "ds_read_b64_tr_b16 v[204:205], %5 offset:38464\n"
        "ds_read_b64_tr_b16 v[206:207], %5 offset:38848\n"
        "s_mov_b32 s40, 32768\n"
        "buffer_load_dwordx4 v[240:243], %4, %7, s40 offen offset:0 sc1\n"
        "s_waitcnt lgkmcnt(8)\n"
        "v_mfma_f32_32x32x16_bf16 %1, v[188:191], v[208:211], %1\n"
        "ds_read_b64_tr_b16 v[184:185], %5 offset:39936\n"
        "ds_read_b64_tr_b16 v[186:187], %5 offset:40320\n"
        "s_waitcnt lgkmcnt(8)\n"
        "v_mfma_f32_32x32x16_bf16 %2, v[192:195], v[208:211], %2\n"
        "s_waitcnt vmcnt(7)\n"
        "v_permlane16_swap_b32 v212, v214\n"
        "v_permlane16_swap_b32 v213, v215\n"
        "ds_read_b64_tr_b16 v[188:189], %5 offset:39968\n"
        "ds_read_b64_tr_b16 v[190:191], %5 offset:40352\n"
        "s_waitcnt lgkmcnt(8)\n"
        "v_mfma_f32_32x32x16_bf16 %0, v[196:199], v[212:215], %0\n"
        "ds_read_b64_tr_b16 v[192:193], %5 offset:40000\n"
        "ds_read_b64_tr_b16 v[194:195], %5 offset:40384\n"
        "buffer_load_dwordx4 v[244:247], %4, %7, s40 offen offset:1024 sc1\n"
        "s_waitcnt lgkmcnt(8)\n"
        "v_mfma_f32_32x32x16_bf16 %1, v[200:203], v[212:215], %1\n"
        "ds_read_b64_tr_b16 v[196:197], %5 offset:41472\n"
        "ds_read_b64_tr_b16 v[198:199], %5 offset:41856\n"
        "s_waitcnt lgkmcnt(8)\n"
        "v_mfma_f32_32x32x16_bf16 %2, v[204:207], v[212:215], %2\n"
        "s_waitcnt vmcnt(7)\n"
        "v_permlane16_swap_b32 v216, v218\n"
        "v_permlane16_swap_b32 v217, v219\n"
        "ds_read_b64_tr_b16 v[200:201], %5 offset:41504\n"
        "ds_read_b64_tr_b16 v[202:203], %5 offset:41888\n"
        "s_waitcnt lgkmcnt(8)\n"
        "v_mfma_f32_32x32x16_bf16 %0, v[184:187], v[216:219], %0\n"
        "ds_read_b64_tr_b16 v[204:205], %5 offset:41536\n"
        "ds_read_b64_tr_b16 v[206:207], %5 offset:41920\n"
        "buffer_load_dwordx4 v[248:251], %4, %7, s40 offen offset:2048 sc1\n"
        "s_waitcnt lgkmcnt(8)\n"
        "v_mfma_f32_32x32x16_bf16 %1, v[188:191], v[216:219], %1\n"
        "ds_read_b64_tr_b16 v[184:185], %5 offset:43008\n"
        "ds_read_b64_tr_b16 v[186:187], %5 offset:43392\n"
        "s_waitcnt lgkmcnt(8)\n"
        "v_mfma_f32_32x32x16_bf16 %2, v[192:195], v[216:219], %2\n"
        "s_waitcnt vmcnt(7)\n"
        "v_permlane16_swap_b32 v220, v222\n"
        "v_permlane16_swap_b32 v221, v223\n"
        "ds_read_b64_tr_b16 v[188:189], %5 offset:43040\n"
        "ds_read_b64_tr_b16 v[190:191], %5 offset:43424\n"
        "s_waitcnt lgkmcnt(8)\n"
        "v_mfma_f32_32x32x16_bf16 %0, v[196:199], v[220:223], %0\n"
        "ds_read_b64_tr_b16 v[192:193], %5 offset:43072\n"
        "ds_read_b64_tr_b16 v[194:195], %5 offset:43456\n"
        "buffer_load_dwordx4 v[252:255], %4, %7, s40 offen offset:3072 sc1\n"
        "s_waitcnt lgkmcnt(8)\n"
        "v_mfma_f32_32x32x16_bf16 %1, v[200:203], v[220:223], %1\n"
        "ds_read_b64_tr_b16 v[196:197], %5 offset:44544\n"
        "ds_read_b64_tr_b16 v[198:199], %5 offset:44928\n"
        "s_waitcnt lgkmcnt(8)\n"
        "v_mfma_f32_32x32x16_bf16 %2, v[204:207], v[220:223], %2\n"
        "s_waitcnt vmcnt(7)\n"
        "v_permlane16_swap_b32 v224, v226\n"
        "v_permlane16_swap_b32 v225, v227\n"
        "ds_read_b64_tr_b16 v[200:201], %5 offset:44576\n"
        "ds_read_b64_tr_b16 v[202:203], %5 offset:44960\n"
        "s_waitcnt lgkmcnt(8)\n"
        "v_mfma_f32_32x32x16_bf16 %0, v[184:187], v[224:227], %0\n"
        "ds_read_b64_tr_b16 v[204:205], %5 offset:44608\n"
        "ds_read_b64_tr_b16 v[206:207], %5 offset:44992\n"
        "s_mov_b32 s40, 36864\n"
        "buffer_load_dwordx4 v[208:211], %4, %7, s40 offen offset:0 sc1\n"
        "s_waitcnt lgkmcnt(8)\n"
        "v_mfma_f32_32x32x16_bf16 %1, v[188:191], v[224:227], %1\n"
        "ds_read_b64_tr_b16 v[184:185], %5 offset:46080\n"
        "ds_read_b64_tr_b16 v[186:187], %5 offset:46464\n"
        "s_waitcnt lgkmcnt(8)\n"
        "v_mfma_f32_32x32x16_bf16 %2, v[192:195], v[224:227], %2\n"
        "s_waitcnt vmcnt(7)\n"
        "v_permlane16_swap_b32 v228, v230\n"
        "v_permlane16_swap_b32 v229, v231\n"
        "ds_read_b64_tr_b16 v[188:189], %5 offset:46112\n"
        "ds_read_b64_tr_b16 v[190:191], %5 offset:46496\n"
        "s_waitcnt lgkmcnt(8)\n"
        "v_mfma_f32_32x32x16_bf16 %0, v[196:199], v[228:231], %0\n"
        "ds_read_b64_tr_b16 v[192:193], %5 offset:46144\n"
        "ds_read_b64_tr_b16 v[194:195], %5 offset:46528\n"
        "buffer_load_dwordx4 v[212:215], %4, %7, s40 offen offset:1024 sc1\n"
        "s_waitcnt lgkmcnt(8)\n"
        "v_mfma_f32_32x32x16_bf16 %1, v[200:203], v[228:231], %1\n"
        "ds_read_b64_tr_b16 v[196:197], %5 offset:47616\n"
        "ds_read_b64_tr_b16 v[198:199], %5 offset:48000\n"
        "s_waitcnt lgkmcnt(8)\n"
        "v_mfma_f32_32x32x16_bf16 %2, v[204:207], v[228:231], %2\n"
        "s_waitcnt vmcnt(7)\n"
        "v_permlane16_swap_b32 v232, v234\n"
        "v_permlane16_swap_b32 v233, v235\n"
        "ds_read_b64_tr_b16 v[200:201], %5 offset:47648\n"
        "ds_read_b64_tr_b16 v[202:203], %5 offset:48032\n"
        "s_waitcnt lgkmcnt(8)\n"
        "v_mfma_f32_32x32x16_bf16 %0, v[184:187], v[232:235], %0\n"
        "ds_read_b64_tr_b16 v[204:205], %5 offset:47680\n"
        "ds_read_b64_tr_b16 v[206:207], %5 offset:48064\n"
        "buffer_load_dwordx4 v[216:219], %4, %7, s40 offen offset:2048 sc1\n"
        "s_waitcnt lgkmcnt(8)\n"
        "v_mfma_f32_32x32x16_bf16 %1, v[188:191], v[232:235], %1\n"
        "ds_read_b64_tr_b16 v[184:185], %5 offset:49152\n"
        "ds_read_b64_tr_b16 v[186:187], %5 offset:49536\n"
        "s_waitcnt lgkmcnt(8)\n"
        "v_mfma_f32_32x32x16_bf16 %2, v[192:195], v[232:235], %2\n"
        "s_waitcnt vmcnt(7)\n"
        "v_permlane16_swap_b32 v236, v238\n"
        "v_permlane16_swap_b32 v237, v239\n"
        "ds_read_b64_tr_b16 v[188:189], %5 offset:49184\n"
        "ds_read_b64_tr_b16 v[190:191], %5 offset:49568\n"
        "s_waitcnt lgkmcnt(8)\n"
        "v_mfma_f32_32x32x16_bf16 %0, v[196:199], v[236:239], %0\n"
        "ds_read_b64_tr_b16 v[192:193], %5 offset:49216\n"
        "ds_read_b64_tr_b16 v[194:195], %5 offset:49600\n"
        "buffer_load_dwordx4 v[220:223], %4, %7, s40 offen offset:3072 sc1\n"
        "s_waitcnt lgkmcnt(8)\n"
        "v_mfma_f32_32x32x16_bf16 %1, v[200:203], v[236:239], %1\n"
        "ds_read_b64_tr_b16 v[196:197], %5 offset:50688\n"
        "ds_read_b64_tr_b16 v[198:199], %5 offset:51072\n"
        "s_waitcnt lgkmcnt(8)\n"
        "v_mfma_f32_32x32x16_bf16 %2, v[204:207], v[236:239], %2\n"
        "s_waitcnt vmcnt(7)\n"
        "v_permlane16_swap_b32 v240, v242\n"
        "v_permlane16_swap_b32 v241, v243\n"
        "ds_read_b64_tr_b16 v[200:201], %5 offset:50720\n"
        "ds_read_b64_tr_b16 v[202:203], %5 offset:51104\n"
        "s_waitcnt lgkmcnt(8)\n"
        "v_mfma_f32_32x32x16_bf16 %0, v[184:187], v[240:243], %0\n"
        "ds_read_b64_tr_b16 v[204:205], %5 offset:50752\n"
        "ds_read_b64_tr_b16 v[206:207], %5 offset:51136\n"
        "s_waitcnt lgkmcnt(8)\n"
        "v_mfma_f32_32x32x16_bf16 %1, v[188:191], v[240:243], %1\n"
        "ds_read_b64_tr_b16 v[184:185], %5 offset:52224\n"
        "ds_read_b64_tr_b16 v[186:187], %5 offset:52608\n"
        "s_waitcnt lgkmcnt(8)\n"
        "v_mfma_f32_32x32x16_bf16 %2, v[192:195], v[240:243], %2\n"
        "s_waitcnt vmcnt(6)\n"
        "v_permlane16_swap_b32 v244, v246\n"
        "v_permlane16_swap_b32 v245, v247\n"
        "ds_read_b64_tr_b16 v[188:189], %5 offset:52256\n"
        "ds_read_b64_tr_b16 v[190:191], %5 offset:52640\n"
        "s_waitcnt lgkmcnt(8)\n"
        "v_mfma_f32_32x32x16_bf16 %0, v[196:199], v[244:247], %0\n"
        "ds_read_b64_tr_b16 v[192:193], %5 offset:52288\n"
        "ds_read_b64_tr_b16 v[194:195], %5 offset:52672\n"
        "s_waitcnt lgkmcnt(8)\n"
        "v_mfma_f32_32x32x16_bf16 %1, v[200:203], v[244:247], %1\n"
        "ds_read_b64_tr_b16 v[196:197], %5 offset:53760\n"
        "ds_read_b64_tr_b16 v[198:199], %5 offset:54144\n"
        "s_waitcnt lgkmcnt(8)\n"
        "v_mfma_f32_32x32x16_bf16 %2, v[204:207], v[244:247], %2\n"
        "s_waitcnt vmcnt(5)\n"
        "v_permlane16_swap_b32 v248, v250\n"
        "v_permlane16_swap_b32 v249, v251\n"
        "ds_read_b64_tr_b16 v[200:201], %5 offset:53792\n"
        "ds_read_b64_tr_b16 v[202:203], %5 offset:54176\n"
        "s_waitcnt lgkmcnt(8)\n"
        "v_mfma_f32_32x32x16_bf16 %0, v[184:187], v[248:251], %0\n"
        "ds_read_b64_tr_b16 v[204:205], %5 offset:53824\n"
        "ds_read_b64_tr_b16 v[206:207], %5 offset:54208\n"
        "buffer_load_dwordx2 %3, %6, %7, 0 offen sc1\n"
        "s_waitcnt lgkmcnt(8)\n"
        "v_mfma_f32_32x32x16_bf16 %1, v[188:191], v[248:251], %1\n"
        "ds_read_b64_tr_b16 v[184:185], %5 offset:55296\n"
        "ds_read_b64_tr_b16 v[186:187], %5 offset:55680\n"
        "s_waitcnt lgkmcnt(8)\n"
        "v_mfma_f32_32x32x16_bf16 %2, v[192:195], v[248:251], %2\n"
        "s_waitcnt vmcnt(5)\n"
        "v_permlane16_swap_b32 v252, v254\n"
        "v_permlane16_swap_b32 v253, v255\n"
        "ds_read_b64_tr_b16 v[188:189], %5 offset:55328\n"
        "ds_read_b64_tr_b16 v[190:191], %5 offset:55712\n"
        "s_waitcnt lgkmcnt(8)\n"
        "v_mfma_f32_32x32x16_bf16 %0, v[196:199], v[252:255], %0\n"
        "ds_read_b64_tr_b16 v[192:193], %5 offset:55360\n"
        "ds_read_b64_tr_b16 v[194:195], %5 offset:55744\n"
        "s_waitcnt lgkmcnt(8)\n"
        "v_mfma_f32_32x32x16_bf16 %1, v[200:203], v[252:255], %1\n"
        "ds_read_b64_tr_b16 v[196:197], %5 offset:56832\n"
        "ds_read_b64_tr_b16 v[198:199], %5 offset:57216\n"
        "s_waitcnt lgkmcnt(8)\n"
        "v_mfma_f32_32x32x16_bf16 %2, v[204:207], v[252:255], %2\n"
        "s_waitcnt vmcnt(4)\n"
        "v_permlane16_swap_b32 v208, v210\n"
        "v_permlane16_swap_b32 v209, v211\n"
        "ds_read_b64_tr_b16 v[200:201], %5 offset:56864\n"
        "ds_read_b64_tr_b16 v[202:203], %5 offset:57248\n"
        "s_waitcnt lgkmcnt(8)\n"
        "v_mfma_f32_32x32x16_bf16 %0, v[184:187], v[208:211], %0\n"
        "ds_read_b64_tr_b16 v[204:205], %5 offset:56896\n"
        "ds_read_b64_tr_b16 v[206:207], %5 offset:57280\n"
        "s_waitcnt lgkmcnt(8)\n"
        "v_mfma_f32_32x32x16_bf16 %1, v[188:191], v[208:211], %1\n"
        "ds_read_b64_tr_b16 v[184:185], %5 offset:58368\n"
        "ds_read_b64_tr_b16 v[186:187], %5 offset:58752\n"
        "s_waitcnt lgkmcnt(8)\n"
        "v_mfma_f32_32x32x16_bf16 %2, v[192:195], v[208:211], %2\n"
        "s_waitcnt vmcnt(3)\n"
        "v_permlane16_swap_b32 v212, v214\n"
        "v_permlane16_swap_b32 v213, v215\n"
        "ds_read_b64_tr_b16 v[188:189], %5 offset:58400\n"
        "ds_read_b64_tr_b16 v[190:191], %5 offset:58784\n"
        "s_waitcnt lgkmcnt(8)\n"
        "v_mfma_f32_32x32x16_bf16 %0, v[196:199], v[212:215], %0\n"
        "ds_read_b64_tr_b16 v[192:193], %5 offset:58432\n"
        "ds_read_b64_tr_b16 v[194:195], %5 offset:58816\n"
        "s_waitcnt lgkmcnt(8)\n"
        "v_mfma_f32_32x32x16_bf16 %1, v[200:203], v[212:215], %1\n"
        "ds_read_b64_tr_b16 v[196:197], %5 offset:59904\n"
        "ds_read_b64_tr_b16 v[198:199], %5 offset:60288\n"
        "s_waitcnt lgkmcnt(8)\n"
        "v_mfma_f32_32x32x16_bf16 %2, v[204:207], v[212:215], %2\n"
        "s_waitcnt vmcnt(2)\n"
        "v_permlane16_swap_b32 v216, v218\n"
        "v_permlane16_swap_b32 v217, v219\n"
        "ds_read_b64_tr_b16 v[200:201], %5 offset:59936\n"
        "ds_read_b64_tr_b16 v[202:203], %5 offset:60320\n"
        "s_waitcnt lgkmcnt(8)\n"
        "v_mfma_f32_32x32x16_bf16 %0, v[184:187], v[216:219], %0\n"
        "ds_read_b64_tr_b16 v[204:205], %5 offset:59968\n"
        "ds_read_b64_tr_b16 v[206:207], %5 offset:60352\n"
        "s_waitcnt lgkmcnt(8)\n"
        "v_mfma_f32_32x32x16_bf16 %1, v[188:191], v[216:219], %1\n"
        "s_waitcnt lgkmcnt(6)\n"
        "v_mfma_f32_32x32x16_bf16 %2, v[192:195], v[216:219], %2\n"
        "s_waitcnt vmcnt(1)\n"
        "v_permlane16_swap_b32 v220, v222\n"
        "v_permlane16_swap_b32 v221, v223\n"
        "s_nop 1\n"
        "s_waitcnt lgkmcnt(4)\n"
        "v_mfma_f32_32x32x16_bf16 %0, v[196:199], v[220:223], %0\n"
        "s_waitcnt lgkmcnt(2)\n"
        "v_mfma_f32_32x32x16_bf16 %1, v[200:203], v[220:223], %1\n"
        "s_waitcnt lgkmcnt(0)\n"
        "v_mfma_f32_32x32x16_bf16 %2, v[204:207], v[220:223], %2\n"
        "s_waitcnt vmcnt(0)\n"
        "s_nop 7\n"
        "s_nop 3\n"
        : "=&v"(acc[0]), "=&v"(acc[1]), "=&v"(acc[2]), "=&v"(st2)
        : "v"(hoff), "v"(fb), "v"(soff), "s"(wsr)
        : "memory", "s40", "v184", "v185", "v186", "v187", "v188", "v189", "v190", "v191", "v192", "v193", "v194", "v195", "v196", "v197", "v198", "v199", "v200", "v201", "v202", "v203", "v204", "v205", "v206", "v207", "v208", "v209", "v210", "v211", "v212", "v213", "v214", "v215", "v216", "v217", "v218", "v219", "v220", "v221", "v222", "v223", "v224", "v225", "v226", "v227", "v228", "v229", "v230", "v231", "v232", "v233", "v234", "v235", "v236", "v237", "v238", "v239", "v240", "v241", "v242", "v243", "v244", "v245", "v246", "v247", "v248", "v249", "v250", "v251", "v252", "v253", "v254", "v255");

                } else {
    asm volatile(
        "s_mov_b32 s40, 0\n"
        "buffer_load_dwordx4 v[176:179], %4, %7, s40 offen offset:0 sc1\n"
        "buffer_load_dwordx4 v[180:183], %4, %7, s40 offen offset:1024 sc1\n"
        "buffer_load_dwordx4 v[184:187], %4, %7, s40 offen offset:2048 sc1\n"
        "buffer_load_dwordx4 v[188:191], %4, %7, s40 offen offset:3072 sc1\n"
        "s_mov_b32 s40, 4096\n"
        "buffer_load_dwordx4 v[192:195], %4, %7, s40 offen offset:0 sc1\n"
        "buffer_load_dwordx4 v[196:199], %4, %7, s40 offen offset:1024 sc1\n"
        "buffer_load_dwordx4 v[200:203], %4, %7, s40 offen offset:2048 sc1\n"
        "buffer_load_dwordx4 v[204:207], %4, %7, s40 offen offset:3072 sc1\n"
        "ds_read_b64_tr_b16 v[152:153], %5 offset:0\n"
        "ds_read_b64_tr_b16 v[154:155], %5 offset:384\n"
        "ds_read_b64_tr_b16 v[156:157], %5 offset:32\n"
        "ds_read_b64_tr_b16 v[158:159], %5 offset:416\n"
        "ds_read_b64_tr_b16 v[160:161], %5 offset:64\n"
        "ds_read_b64_tr_b16 v[162:163], %5 offset:448\n"
        "ds_read_b64_tr_b16 v[164:165], %5 offset:1536\n"
        "ds_read_b64_tr_b16 v[166:167], %5 offset:1920\n"
        "ds_read_b64_tr_b16 v[168:169], %5 offset:1568\n"
        "ds_read_b64_tr_b16 v[170:171], %5 offset:1952\n"
        "s_waitcnt vmcnt(7)\n"
        "v_permlane16_swap_b32 v176, v178\n"
        "v_permlane16_swap_b32 v177, v179\n"
        "s_nop 0\n"
        "s_waitcnt lgkmcnt(8)\n"
        "v_mfma_f32_32x32x16_bf16 %0, v[152:155], v[176:179], 0\n"
        "ds_read_b64_tr_b16 v[172:173], %5 offset:1600\n"
        "ds_read_b64_tr_b16 v[174:175], %5 offset:1984\n"
        "s_mov_b32 s40, 8192\n"
        "buffer_load_dwordx4 v[208:211], %4, %7, s40 offen offset:0 sc1\n"
        "s_waitcnt lgkmcnt(8)\n"
        "v_mfma_f32_32x32x16_bf16 %1, v[156:159], v[176:179], 0\n"
        "ds_read_b64_tr_b16 v[152:153], %5 offset:3072\n"
        "ds_read_b64_tr_b16 v[154:155], %5 offset:3456\n"
        "s_waitcnt lgkmcnt(8)\n"
        "v_mfma_f32_32x32x16_bf16 %2, v[160:163], v[176:179], 0\n"
        "s_waitcnt vmcnt(7)\n"
        "v_permlane16_swap_b32 v180, v182\n"
        "v_permlane16_swap_b32 v181, v183\n"
        "ds_read_b64_tr_b16 v[156:157], %5 offset:3104\n"
        "ds_read_b64_tr_b16 v[158:159], %5 offset:3488\n"
        "s_waitcnt lgkmcnt(8)\n"
        "v_mfma_f32_32x32x16_bf16 %0, v[164:167], v[180:183], %0\n"
        "ds_read_b64_tr_b16 v[160:161], %5 offset:3136\n"
        "ds_read_b64_tr_b16 v[162:163], %5 offset:3520\n"
        "buffer_load_dwordx4 v[212:215], %4, %7, s40 offen offset:1024 sc1\n"
        "s_waitcnt lgkmcnt(8)\n"
        "v_mfma_f32_32x32x16_bf16 %1, v[168:171], v[180:183], %1\n"
        "ds_read_b64_tr_b16 v[164:165], %5 offset:4608\n"
        "ds_read_b64_tr_b16 v[166:167], %5 offset:4992\n"
        "s_waitcnt lgkmcnt(8)\n"
        "v_mfma_f32_32x32x16_bf16 %2, v[172:175], v[180:183], %2\n"
        "s_waitcnt vmcnt(7)\n"
        "v_permlane16_swap_b32 v184, v186\n"
        "v_permlane16_swap_b32 v185, v187\n"
        "ds_read_b64_tr_b16 v[168:169], %5 offset:4640\n"
        "ds_read_b64_tr_b16 v[170:171], %5 offset:5024\n"
        "s_waitcnt lgkmcnt(8)\n"
        "v_mfma_f32_32x32x16_bf16 %0, v[152:155], v[184:187], %0\n"
        "ds_read_b64_tr_b16 v[172:173], %5 offset:4672\n"
        "ds_read_b64_tr_b16 v[174:175], %5 offset:5056\n"
        "buffer_load_dwordx4 v[216:219], %4, %7, s40 offen offset:2048 sc1\n"
        "s_waitcnt lgkmcnt(8)\n"
        "v_mfma_f32_32x32x16_bf16 %1, v[156:159], v[184:187], %1\n"
        "ds_read_b64_tr_b16 v[152:153], %5 offset:6144\n"
        "ds_read_b64_tr_b16 v[154:155], %5 offset:6528\n"
        "s_waitcnt lgkmcnt(8)\n"
        "v_mfma_f32_32x32x16_bf16 %2, v[160:163], v[184:187], %2\n"
        "s_waitcnt vmcnt(7)\n"
        "v_permlane16_swap_b32 v188, v190\n"
        "v_permlane16_swap_b32 v189, v191\n"
        "ds_read_b64_tr_b16 v[156:157], %5 offset:6176\n"
        "ds_read_b64_tr_b16 v[158:159], %5 offset:6560\n"
        "s_waitcnt lgkmcnt(8)\n"
        "v_mfma_f32_32x32x16_bf16 %0, v[164:167], v[188:191], %0\n"
        "ds_read_b64_tr_b16 v[160:161], %5 offset:6208\n"
        "ds_read_b64_tr_b16 v[162:163], %5 offset:6592\n"
        "buffer_load_dwordx4 v[220:223], %4, %7, s40 offen offset:3072 sc1\n"
        "s_waitcnt lgkmcnt(8)\n"
        "v_mfma_f32_32x32x16_bf16 %1, v[168:171], v[188:191], %1\n"
        "ds_read_b64_tr_b16 v[164:165], %5 offset:7680\n"
        "ds_read_b64_tr_b16 v[166:167], %5 offset:8064\n"
        "s_waitcnt lgkmcnt(8)\n"
        "v_mfma_f32_32x32x16_bf16 %2, v[172:175], v[188:191], %2\n"
        "s_waitcnt vmcnt(7)\n"
        "v_permlane16_swap_b32 v192, v194\n"
        "v_permlane16_swap_b32 v193, v195\n"
        "ds_read_b64_tr_b16 v[168:169], %5 offset:7712\n"
        "ds_read_b64_tr_b16 v[170:171], %5 offset:8096\n"
        "s_waitcnt lgkmcnt(8)\n"
        "v_mfma_f32_32x32x16_bf16 %0, v[152:155], v[192:195], %0\n"
        "ds_read_b64_tr_b16 v[172:173], %5 offset:7744\n"
        "ds_read_b64_tr_b16 v[174:175], %5 offset:8128\n"
        "s_mov_b32 s40, 12288\n"
        "buffer_load_dwordx4 v[224:227], %4, %7, s40 offen offset:0 sc1\n"
        "s_waitcnt lgkmcnt(8)\n"
        "v_mfma_f32_32x32x16_bf16 %1, v[156:159], v[192:195], %1\n"
        "ds_read_b64_tr_b16 v[152:153], %5 offset:9216\n"
        "ds_read_b64_tr_b16 v[154:155], %5 offset:9600\n"
        "s_waitcnt lgkmcnt(8)\n"
        "v_mfma_f32_32x32x16_bf16 %2, v[160:163], v[192:195], %2\n"
        "s_waitcnt vmcnt(7)\n"
        "v_permlane16_swap_b32 v196, v198\n"
        "v_permlane16_swap_b32 v197, v199\n"
        "ds_read_b64_tr_b16 v[156:157], %5 offset:9248\n"
        "ds_read_b64_tr_b16 v[158:159], %5 offset:9632\n"
        "s_waitcnt lgkmcnt(8)\n"
        "v_mfma_f32_32x32x16_bf16 %0, v[164:167], v[196:199], %0\n"
        "ds_read_b64_tr_b16 v[160:161], %5 offset:9280\n"
        "ds_read_b64_tr_b16 v[162:163], %5 offset:9664\n"
        "buffer_load_dwordx4 v[228:231], %4, %7, s40 offen offset:1024 sc1\n"
        "s_waitcnt lgkmcnt(8)\n"
        "v_mfma_f32_32x32x16_bf16 %1, v[168:171], v[196:199], %1\n"
        "ds_read_b64_tr_b16 v[164:165], %5 offset:10752\n"
        "ds_read_b64_tr_b16 v[166:167], %5 offset:11136\n"
        "s_waitcnt lgkmcnt(8)\n"
        "v_mfma_f32_32x32x16_bf16 %2, v[172:175], v[196:199], %2\n"
        "s_waitcnt vmcnt(7)\n"
        "v_permlane16_swap_b32 v200, v202\n"
        "v_permlane16_swap_b32 v201, v203\n"
        "ds_read_b64_tr_b16 v[168:169], %5 offset:10784\n"
        "ds_read_b64_tr_b16 v[170:171], %5 offset:11168\n"
        "s_waitcnt lgkmcnt(8)\n"
        "v_mfma_f32_32x32x16_bf16 %0, v[152:155], v[200:203], %0\n"
        "ds_read_b64_tr_b16 v[172:173], %5 offset:10816\n"
        "ds_read_b64_tr_b16 v[174:175], %5 offset:11200\n"
        "buffer_load_dwordx4 v[232:235], %4, %7, s40 offen offset:2048 sc1\n"
        "s_waitcnt lgkmcnt(8)\n"
        "v_mfma_f32_32x32x16_bf16 %1, v[156:159], v[200:203], %1\n"
        "ds_read_b64_tr_b16 v[152:153], %5 offset:12288\n"
        "ds_read_b64_tr_b16 v[154:155], %5 offset:12672\n"
        "s_waitcnt lgkmcnt(8)\n"
        "v_mfma_f32_32x32x16_bf16 %2, v[160:163], v[200:203], %2\n"
        "s_waitcnt vmcnt(7)\n"
        "v_permlane16_swap_b32 v204, v206\n"
        "v_permlane16_swap_b32 v205, v207\n"
        "ds_read_b64_tr_b16 v[156:157], %5 offset:12320\n"
        "ds_read_b64_tr_b16 v[158:159], %5 offset:12704\n"
        "s_waitcnt lgkmcnt(8)\n"
        "v_mfma_f32_32x32x16_bf16 %0, v[164:167], v[204:207], %0\n"
        "ds_read_b64_tr_b16 v[160:161], %5 offset:12352\n"
        "ds_read_b64_tr_b16 v[162:163], %5 offset:12736\n"
        "buffer_load_dwordx4 v[236:239], %4, %7, s40 offen offset:3072 sc1\n"
        "s_waitcnt lgkmcnt(8)\n"
        "v_mfma_f32_32x32x16_bf16 %1, v[168:171], v[204:207], %1\n"
        "ds_read_b64_tr_b16 v[164:165], %5 offset:13824\n"
        "ds_read_b64_tr_b16 v[166:167], %5 offset:14208\n"
        "s_waitcnt lgkmcnt(8)\n"
        "v_mfma_f32_32x32x16_bf16 %2, v[172:175], v[204:207], %2\n"
        "s_waitcnt vmcnt(7)\n"
        "v_permlane16_swap_b32 v208, v210\n"
        "v_permlane16_swap_b32 v209, v211\n"
        "ds_read_b64_tr_b16 v[168:169], %5 offset:13856\n"
        "ds_read_b64_tr_b16 v[170:171], %5 offset:14240\n"
        "s_waitcnt lgkmcnt(8)\n"
        "v_mfma_f32_32x32x16_bf16 %0, v[152:155], v[208:211], %0\n"
        "ds_read_b64_tr_b16 v[172:173], %5 offset:13888\n"
        "ds_read_b64_tr_b16 v[174:175], %5 offset:14272\n"
        "s_mov_b32 s40, 16384\n"
        "buffer_load_dwordx4 v[240:243], %4, %7, s40 offen offset:0 sc1\n"
        "s_waitcnt lgkmcnt(8)\n"
        "v_mfma_f32_32x32x16_bf16 %1, v[156:159], v[208:211], %1\n"
        "ds_read_b64_tr_b16 v[152:153], %5 offset:15360\n"
        "ds_read_b64_tr_b16 v[154:155], %5 offset:15744\n"
        "s_waitcnt lgkmcnt(8)\n"
        "v_mfma_f32_32x32x16_bf16 %2, v[160:163], v[208:211], %2\n"
        "s_waitcnt vmcnt(7)\n"
        "v_permlane16_swap_b32 v212, v214\n"
        "v_permlane16_swap_b32 v213, v215\n"
        "ds_read_b64_tr_b16 v[156:157], %5 offset:15392\n"
        "ds_read_b64_tr_b16 v[158:159], %5 offset:15776\n"
        "s_waitcnt lgkmcnt(8)\n"
        "v_mfma_f32_32x32x16_bf16 %0, v[164:167], v[212:215], %0\n"
        "ds_read_b64_tr_b16 v[160:161], %5 offset:15424\n"
        "ds_read_b64_tr_b16 v[162:163], %5 offset:15808\n"
        "buffer_load_dwordx4 v[244:247], %4, %7, s40 offen offset:1024 sc1\n"
        "s_waitcnt lgkmcnt(8)\n"
        "v_mfma_f32_32x32x16_bf16 %1, v[168:171], v[212:215], %1\n"
        "ds_read_b64_tr_b16 v[164:165], %5 offset:16896\n"
        "ds_read_b64_tr_b16 v[166:167], %5 offset:17280\n"
        "s_waitcnt lgkmcnt(8)\n"
        "v_mfma_f32_32x32x16_bf16 %2, v[172:175], v[212:215], %2\n"
        "s_waitcnt vmcnt(7)\n"
        "v_permlane16_swap_b32 v216, v218\n"
        "v_permlane16_swap_b32 v217, v219\n"
        "ds_read_b64_tr_b16 v[168:169], %5 offset:16928\n"
        "ds_read_b64_tr_b16 v[170:171], %5 offset:17312\n"
        "s_waitcnt lgkmcnt(8)\n"
        "v_mfma_f32_32x32x16_bf16 %0, v[152:155], v[216:219], %0\n"
        "ds_read_b64_tr_b16 v[172:173], %5 offset:16960\n"
        "ds_read_b64_tr_b16 v[174:175], %5 offset:17344\n"
        "buffer_load_dwordx4 v[248:251], %4, %7, s40 offen offset:2048 sc1\n"
        "s_waitcnt lgkmcnt(8)\n"
        "v_mfma_f32_32x32x16_bf16 %1, v[156:159], v[216:219], %1\n"
        "ds_read_b64_tr_b16 v[152:153], %5 offset:18432\n"
        "ds_read_b64_tr_b16 v[154:155], %5 offset:18816\n"
        "s_waitcnt lgkmcnt(8)\n"
        "v_mfma_f32_32x32x16_bf16 %2, v[160:163], v[216:219], %2\n"
        "s_waitcnt vmcnt(7)\n"
        "v_permlane16_swap_b32 v220, v222\n"
        "v_permlane16_swap_b32 v221, v223\n"
        "ds_read_b64_tr_b16 v[156:157], %5 offset:18464\n"
        "ds_read_b64_tr_b16 v[158:159], %5 offset:18848\n"
        "s_waitcnt lgkmcnt(8)\n"
        "v_mfma_f32_32x32x16_bf16 %0, v[164:167], v[220:223], %0\n"
        "ds_read_b64_tr_b16 v[160:161], %5 offset:18496\n"
        "ds_read_b64_tr_b16 v[162:163], %5 offset:18880\n"
        "buffer_load_dwordx4 v[252:255], %4, %7, s40 offen offset:3072 sc1\n"
        "s_waitcnt lgkmcnt(8)\n"
        "v_mfma_f32_32x32x16_bf16 %1, v[168:171], v[220:223], %1\n"
        "ds_read_b64_tr_b16 v[164:165], %5 offset:19968\n"
        "ds_read_b64_tr_b16 v[166:167], %5 offset:20352\n"
        "s_waitcnt lgkmcnt(8)\n"
        "v_mfma_f32_32x32x16_bf16 %2, v[172:175], v[220:223], %2\n"
        "s_waitcnt vmcnt(7)\n"
        "v_permlane16_swap_b32 v224, v226\n"
        "v_permlane16_swap_b32 v225, v227\n"
        "ds_read_b64_tr_b16 v[168:169], %5 offset:20000\n"
        "ds_read_b64_tr_b16 v[170:171], %5 offset:20384\n"
        "s_waitcnt lgkmcnt(8)\n"
        "v_mfma_f32_32x32x16_bf16 %0, v[152:155], v[224:227], %0\n"
        "ds_read_b64_tr_b16 v[172:173], %5 offset:20032\n"
        "ds_read_b64_tr_b16 v[174:175], %5 offset:20416\n"
        "s_mov_b32 s40, 20480\n"
        "buffer_load_dwordx4 v[176:179], %4, %7, s40 offen offset:0 sc1\n"
        "s_waitcnt lgkmcnt(8)\n"
        "v_mfma_f32_32x32x16_bf16 %1, v[156:159], v[224:227], %1\n"
        "ds_read_b64_tr_b16 v[152:153], %5 offset:21504\n"
        "ds_read_b64_tr_b16 v[154:155], %5 offset:21888\n"
        "s_waitcnt lgkmcnt(8)\n"
        "v_mfma_f32_32x32x16_bf16 %2, v[160:163], v[224:227], %2\n"
        "s_waitcnt vmcnt(7)\n"
        "v_permlane16_swap_b32 v228, v230\n"
        "v_permlane16_swap_b32 v229, v231\n"
        "ds_read_b64_tr_b16 v[156:157], %5 offset:21536\n"
        "ds_read_b64_tr_b16 v[158:159], %5 offset:21920\n"
        "s_waitcnt lgkmcnt(8)\n"
        "v_mfma_f32_32x32x16_bf16 %0, v[164:167], v[228:231], %0\n"
        "ds_read_b64_tr_b16 v[160:161], %5 offset:21568\n"
        "ds_read_b64_tr_b16 v[162:163], %5 offset:21952\n"
        "buffer_load_dwordx4 v[180:183], %4, %7, s40 offen offset:1024 sc1\n"
        "s_waitcnt lgkmcnt(8)\n"
        "v_mfma_f32_32x32x16_bf16 %1, v[168:171], v[228:231], %1\n"
        "ds_read_b64_tr_b16 v[164:165], %5 offset:23040\n"
        "ds_read_b64_tr_b16 v[166:167], %5 offset:23424\n"
        "s_waitcnt lgkmcnt(8)\n"
        "v_mfma_f32_32x32x16_bf16 %2, v[172:175], v[228:231], %2\n"
        "s_waitcnt vmcnt(7)\n"
        "v_permlane16_swap_b32 v232, v234\n"
        "v_permlane16_swap_b32 v233, v235\n"
        "ds_read_b64_tr_b16 v[168:169], %5 offset:23072\n"
        "ds_read_b64_tr_b16 v[170:171], %5 offset:23456\n"
        "s_waitcnt lgkmcnt(8)\n"
        "v_mfma_f32_32x32x16_bf16 %0, v[152:155], v[232:235], %0\n"
        "ds_read_b64_tr_b16 v[172:173], %5 offset:23104\n"
        "ds_read_b64_tr_b16 v[174:175], %5 offset:23488\n"
        "buffer_load_dwordx4 v[184:187], %4, %7, s40 offen offset:2048 sc1\n"
        "s_waitcnt lgkmcnt(8)\n"
        "v_mfma_f32_32x32x16_bf16 %1, v[156:159], v[232:235], %1\n"
        "ds_read_b64_tr_b16 v[152:153], %5 offset:24576\n"
        "ds_read_b64_tr_b16 v[154:155], %5 offset:24960\n"
        "s_waitcnt lgkmcnt(8)\n"
        "v_mfma_f32_32x32x16_bf16 %2, v[160:163], v[232:235], %2\n"
        "s_waitcnt vmcnt(7)\n"
        "v_permlane16_swap_b32 v236, v238\n"
        "v_permlane16_swap_b32 v237, v239\n"
        "ds_read_b64_tr_b16 v[156:157], %5 offset:24608\n"
        "ds_read_b64_tr_b16 v[158:159], %5 offset:24992\n"
        "s_waitcnt lgkmcnt(8)\n"
        "v_mfma_f32_32x32x16_bf16 %0, v[164:167], v[236:239], %0\n"
        "ds_read_b64_tr_b16 v[160:161], %5 offset:24640\n"
        "ds_read_b64_tr_b16 v[162:163], %5 offset:25024\n"
        "buffer_load_dwordx4 v[188:191], %4, %7, s40 offen offset:3072 sc1\n"
        "s_waitcnt lgkmcnt(8)\n"
        "v_mfma_f32_32x32x16_bf16 %1, v[168:171], v[236:239], %1\n"
        "ds_read_b64_tr_b16 v[164:165], %5 offset:26112\n"
        "ds_read_b64_tr_b16 v[166:167], %5 offset:26496\n"
        "s_waitcnt lgkmcnt(8)\n"
        "v_mfma_f32_32x32x16_bf16 %2, v[172:175], v[236:239], %2\n"
        "s_waitcnt vmcnt(7)\n"
        "v_permlane16_swap_b32 v240, v242\n"
        "v_permlane16_swap_b32 v241, v243\n"
        "ds_read_b64_tr_b16 v[168:169], %5 offset:26144\n"
        "ds_read_b64_tr_b16 v[170:171], %5 offset:26528\n"
        "s_waitcnt lgkmcnt(8)\n"
        "v_mfma_f32_32x32x16_bf16 %0, v[152:155], v[240:243], %0\n"
        "ds_read_b64_tr_b16 v[172:173], %5 offset:26176\n"
        "ds_read_b64_tr_b16 v[174:175], %5 offset:26560\n"
        "s_mov_b32 s40, 24576\n"
        "buffer_load_dwordx4 v[192:195], %4, %7, s40 offen offset:0 sc1\n"
        "s_waitcnt lgkmcnt(8)\n"
        "v_mfma_f32_32x32x16_bf16 %1, v[156:159], v[240:243], %1\n"
        "ds_read_b64_tr_b16 v[152:153], %5 offset:27648\n"
        "ds_read_b64_tr_b16 v[154:155], %5 offset:28032\n"
        "s_waitcnt lgkmcnt(8)\n"
        "v_mfma_f32_32x32x16_bf16 %2, v[160:163], v[240:243], %2\n"
        "s_waitcnt vmcnt(7)\n"
        "v_permlane16_swap_b32 v244, v246\n"
        "v_permlane16_swap_b32 v245, v247\n"
        "ds_read_b64_tr_b16 v[156:157], %5 offset:27680\n"
        "ds_read_b64_tr_b16 v[158:159], %5 offset:28064\n"
        "s_waitcnt lgkmcnt(8)\n"
        "v_mfma_f32_32x32x16_bf16 %0, v[164:167], v[244:247], %0\n"
        "ds_read_b64_tr_b16 v[160:161], %5 offset:27712\n"
        "ds_read_b64_tr_b16 v[162:163], %5 offset:28096\n"
        "buffer_load_dwordx4 v[196:199], %4, %7, s40 offen offset:1024 sc1\n"
        "s_waitcnt lgkmcnt(8)\n"
        "v_mfma_f32_32x32x16_bf16 %1, v[168:171], v[244:247], %1\n"
        "ds_read_b64_tr_b16 v[164:165], %5 offset:29184\n"
        "ds_read_b64_tr_b16 v[166:167], %5 offset:29568\n"
        "s_waitcnt lgkmcnt(8)\n"
        "v_mfma_f32_32x32x16_bf16 %2, v[172:175], v[244:247], %2\n"
        "s_waitcnt vmcnt(7)\n"
        "v_permlane16_swap_b32 v248, v250\n"
        "v_permlane16_swap_b32 v249, v251\n"
        "ds_read_b64_tr_b16 v[168:169], %5 offset:29216\n"
        "ds_read_b64_tr_b16 v[170:171], %5 offset:29600\n"
        "s_waitcnt lgkmcnt(8)\n"
        "v_mfma_f32_32x32x16_bf16 %0, v[152:155], v[248:251], %0\n"
        "ds_read_b64_tr_b16 v[172:173], %5 offset:29248\n"
        "ds_read_b64_tr_b16 v[174:175], %5 offset:29632\n"
        "buffer_load_dwordx4 v[200:203], %4, %7, s40 offen offset:2048 sc1\n"
        "s_waitcnt lgkmcnt(8)\n"
        "v_mfma_f32_32x32x16_bf16 %1, v[156:159], v[248:251], %1\n"
        "ds_read_b64_tr_b16 v[152:153], %5 offset:30720\n"
        "ds_read_b64_tr_b16 v[154:155], %5 offset:31104\n"
        "s_waitcnt lgkmcnt(8)\n"
        "v_mfma_f32_32x32x16_bf16 %2, v[160:163], v[248:251], %2\n"
        "s_waitcnt vmcnt(7)\n"
        "v_permlane16_swap_b32 v252, v254\n"
        "v_permlane16_swap_b32 v253, v255\n"
        "ds_read_b64_tr_b16 v[156:157], %5 offset:30752\n"
        "ds_read_b64_tr_b16 v[158:159], %5 offset:31136\n"
        "s_waitcnt lgkmcnt(8)\n"
        "v_mfma_f32_32x32x16_bf16 %0, v[164:167], v[252:255], %0\n"
        "ds_read_b64_tr_b16 v[160:161], %5 offset:30784\n"
        "ds_read_b64_tr_b16 v[162:163], %5 offset:31168\n"
        "buffer_load_dwordx4 v[204:207], %4, %7, s40 offen offset:3072 sc1\n"
        "s_waitcnt lgkmcnt(8)\n"
        "v_mfma_f32_32x32x16_bf16 %1, v[168:171], v[252:255], %1\n"
        "ds_read_b64_tr_b16 v[164:165], %5 offset:32256\n"
        "ds_read_b64_tr_b16 v[166:167], %5 offset:32640\n"
        "s_waitcnt lgkmcnt(8)\n"
        "v_mfma_f32_32x32x16_bf16 %2, v[172:175], v[252:255], %2\n"
        "s_waitcnt vmcnt(7)\n"
        "v_permlane16_swap_b32 v176, v178\n"
        "v_permlane16_swap_b32 v177, v179\n"
        "ds_read_b64_tr_b16 v[168:169], %5 offset:32288\n"
        "ds_read_b64_tr_b16 v[170:171], %5 offset:32672\n"
        "s_waitcnt lgkmcnt(8)\n"
        "v_mfma_f32_32x32x16_bf16 %0, v[152:155], v[176:179], %0\n"
        "ds_read_b64_tr_b16 v[172:173], %5 offset:32320\n"
        "ds_read_b64_tr_b16 v[174:175], %5 offset:32704\n"
        "s_mov_b32 s40, 28672\n"
        "buffer_load_dwordx4 v[208:211], %4, %7, s40 offen offset:0 sc1\n"
        "s_waitcnt lgkmcnt(8)\n"
        "v_mfma_f32_32x32x16_bf16 %1, v[156:159], v[176:179], %1\n"
        "ds_read_b64_tr_b16 v[152:153], %5 offset:33792\n"
        "ds_read_b64_tr_b16 v[154:155], %5 offset:34176\n"
        "s_waitcnt lgkmcnt(8)\n"
        "v_mfma_f32_32x32x16_bf16 %2, v[160:163], v[176:179], %2\n"
        "s_waitcnt vmcnt(7)\n"
        "v_permlane16_swap_b32 v180, v182\n"
        "v_permlane16_swap_b32 v181, v183\n"
        "ds_read_b64_tr_b16 v[156:157], %5 offset:33824\n"
        "ds_read_b64_tr_b16 v[158:159], %5 offset:34208\n"
        "s_waitcnt lgkmcnt(8)\n"
        "v_mfma_f32_32x32x16_bf16 %0, v[164:167], v[180:183], %0\n"
        "ds_read_b64_tr_b16 v[160:161], %5 offset:33856\n"
        "ds_read_b64_tr_b16 v[162:163], %5 offset:34240\n"
        "buffer_load_dwordx4 v[212:215], %4, %7, s40 offen offset:1024 sc1\n"
        "s_waitcnt lgkmcnt(8)\n"
        "v_mfma_f32_32x32x16_bf16 %1, v[168:171], v[180:183], %1\n"
        "ds_read_b64_tr_b16 v[164:165], %5 offset:35328\n"
        "ds_read_b64_tr_b16 v[166:167], %5 offset:35712\n"
        "s_waitcnt lgkmcnt(8)\n"
        "v_mfma_f32_32x32x16_bf16 %2, v[172:175], v[180:183], %2\n"
        "s_waitcnt vmcnt(7)\n"
        "v_permlane16_swap_b32 v184, v186\n"
        "v_permlane16_swap_b32 v185, v187\n"
        "ds_read_b64_tr_b16 v[168:169], %5 offset:35360\n"
        "ds_read_b64_tr_b16 v[170:171], %5 offset:35744\n"
        "s_waitcnt lgkmcnt(8)\n"
        "v_mfma_f32_32x32x16_bf16 %0, v[152:155], v[184:187], %0\n"
        "ds_read_b64_tr_b16 v[172:173], %5 offset:35392\n"
        "ds_read_b64_tr_b16 v[174:175], %5 offset:35776\n"
        "buffer_load_dwordx4 v[216:219], %4, %7, s40 offen offset:2048 sc1\n"
        "s_waitcnt lgkmcnt(8)\n"
        "v_mfma_f32_32x32x16_bf16 %1, v[156:159], v[184:187], %1\n"
        "ds_read_b64_tr_b16 v[152:153], %5 offset:36864\n"
        "ds_read_b64_tr_b16 v[154:155], %5 offset:37248\n"
        "s_waitcnt lgkmcnt(8)\n"
        "v_mfma_f32_32x32x16_bf16 %2, v[160:163], v[184:187], %2\n"
        "s_waitcnt vmcnt(7)\n"
        "v_permlane16_swap_b32 v188, v190\n"
        "v_permlane16_swap_b32 v189, v191\n"
        "ds_read_b64_tr_b16 v[156:157], %5 offset:36896\n"
        "ds_read_b64_tr_b16 v[158:159], %5 offset:37280\n"
        "s_waitcnt lgkmcnt(8)\n"
        "v_mfma_f32_32x32x16_bf16 %0, v[164:167], v[188:191], %0\n"
        "ds_read_b64_tr_b16 v[160:161], %5 offset:36928\n"
        "ds_read_b64_tr_b16 v[162:163], %5 offset:37312\n"
        "buffer_load_dwordx4 v[220:223], %4, %7, s40 offen offset:3072 sc1\n"
        "s_waitcnt lgkmcnt(8)\n"
        "v_mfma_f32_32x32x16_bf16 %1, v[168:171], v[188:191], %1\n"
        "ds_read_b64_tr_b16 v[164:165], %5 offset:38400\n"
        "ds_read_b64_tr_b16 v[166:167], %5 offset:38784\n"
        "s_waitcnt lgkmcnt(8)\n"
        "v_mfma_f32_32x32x16_bf16 %2, v[172:175], v[188:191], %2\n"
        "s_waitcnt vmcnt(7)\n"
        "v_permlane16_swap_b32 v192, v194\n"
        "v_permlane16_swap_b32 v193, v195\n"
        "ds_read_b64_tr_b16 v[168:169], %5 offset:38432\n"
        "ds_read_b64_tr_b16 v[170:171], %5 offset:38816\n"
        "s_waitcnt lgkmcnt(8)\n"
        "v_mfma_f32_32x32x16_bf16 %0, v[152:155], v[192:195], %0\n"
        "ds_read_b64_tr_b16 v[172:173], %5 offset:38464\n"
        "ds_read_b64_tr_b16 v[174:175], %5 offset:38848\n"
        "s_mov_b32 s40, 32768\n"
        "buffer_load_dwordx4 v[224:227], %4, %7, s40 offen offset:0 sc1\n"
        "s_waitcnt lgkmcnt(8)\n"
        "v_mfma_f32_32x32x16_bf16 %1, v[156:159], v[192:195], %1\n"
        "ds_read_b64_tr_b16 v[152:153], %5 offset:39936\n"
        "ds_read_b64_tr_b16 v[154:155], %5 offset:40320\n"
        "s_waitcnt lgkmcnt(8)\n"
        "v_mfma_f32_32x32x16_bf16 %2, v[160:163], v[192:195], %2\n"
        "s_waitcnt vmcnt(7)\n"
        "v_permlane16_swap_b32 v196, v198\n"
        "v_permlane16_swap_b32 v197, v199\n"
        "ds_read_b64_tr_b16 v[156:157], %5 offset:39968\n"
        "ds_read_b64_tr_b16 v[158:159], %5 offset:40352\n"
        "s_waitcnt lgkmcnt(8)\n"
        "v_mfma_f32_32x32x16_bf16 %0, v[164:167], v[196:199], %0\n"
        "ds_read_b64_tr_b16 v[160:161], %5 offset:40000\n"
        "ds_read_b64_tr_b16 v[162:163], %5 offset:40384\n"
        "buffer_load_dwordx4 v[228:231], %4, %7, s40 offen offset:1024 sc1\n"
        "s_waitcnt lgkmcnt(8)\n"
        "v_mfma_f32_32x32x16_bf16 %1, v[168:171], v[196:199], %1\n"
        "ds_read_b64_tr_b16 v[164:165], %5 offset:41472\n"
        "ds_read_b64_tr_b16 v[166:167], %5 offset:41856\n"
        "s_waitcnt lgkmcnt(8)\n"
        "v_mfma_f32_32x32x16_bf16 %2, v[172:175], v[196:199], %2\n"
        "s_waitcnt vmcnt(7)\n"
        "v_permlane16_swap_b32 v200, v202\n"
        "v_permlane16_swap_b32 v201, v203\n"
        "ds_read_b64_tr_b16 v[168:169], %5 offset:41504\n"
        "ds_read_b64_tr_b16 v[170:171], %5 offset:41888\n"
        "s_waitcnt lgkmcnt(8)\n"
        "v_mfma_f32_32x32x16_bf16 %0, v[152:155], v[200:203], %0\n"
        "ds_read_b64_tr_b16 v[172:173], %5 offset:41536\n"
        "ds_read_b64_tr_b16 v[174:175], %5 offset:41920\n"
        "buffer_load_dwordx4 v[232:235], %4, %7, s40 offen offset:2048 sc1\n"
        "s_waitcnt lgkmcnt(8)\n"
        "v_mfma_f32_32x32x16_bf16 %1, v[156:159], v[200:203], %1\n"
        "ds_read_b64_tr_b16 v[152:153], %5 offset:43008\n"
        "ds_read_b64_tr_b16 v[154:155], %5 offset:43392\n"
        "s_waitcnt lgkmcnt(8)\n"
        "v_mfma_f32_32x32x16_bf16 %2, v[160:163], v[200:203], %2\n"
        "s_waitcnt vmcnt(7)\n"
        "v_permlane16_swap_b32 v204, v206\n"
        "v_permlane16_swap_b32 v205, v207\n"
        "ds_read_b64_tr_b16 v[156:157], %5 offset:43040\n"
        "ds_read_b64_tr_b16 v[158:159], %5 offset:43424\n"
        "s_waitcnt lgkmcnt(8)\n"
        "v_mfma_f32_32x32x16_bf16 %0, v[164:167], v[204:207], %0\n"
        "ds_read_b64_tr_b16 v[160:161], %5 offset:43072\n"
        "ds_read_b64_tr_b16 v[162:163], %5 offset:43456\n"
        "buffer_load_dwordx4 v[236:239], %4, %7, s40 offen offset:3072 sc1\n"
        "s_waitcnt lgkmcnt(8)\n"
        "v_mfma_f32_32x32x16_bf16 %1, v[168:171], v[204:207], %1\n"
        "ds_read_b64_tr_b16 v[164:165], %5 offset:44544\n"
        "ds_read_b64_tr_b16 v[166:167], %5 offset:44928\n"
        "s_waitcnt lgkmcnt(8)\n"
        "v_mfma_f32_32x32x16_bf16 %2, v[172:175], v[204:207], %2\n"
        "s_waitcnt vmcnt(7)\n"
        "v_permlane16_swap_b32 v208, v210\n"
        "v_permlane16_swap_b32 v209, v211\n"
        "ds_read_b64_tr_b16 v[168:169], %5 offset:44576\n"
        "ds_read_b64_tr_b16 v[170:171], %5 offset:44960\n"
        "s_waitcnt lgkmcnt(8)\n"
        "v_mfma_f32_32x32x16_bf16 %0, v[152:155], v[208:211], %0\n"
        "ds_read_b64_tr_b16 v[172:173], %5 offset:44608\n"
        "ds_read_b64_tr_b16 v[174:175], %5 offset:44992\n"
        "s_mov_b32 s40, 36864\n"
        "buffer_load_dwordx4 v[240:243], %4, %7, s40 offen offset:0 sc1\n"
        "s_waitcnt lgkmcnt(8)\n"
        "v_mfma_f32_32x32x16_bf16 %1, v[156:159], v[208:211], %1\n"
        "ds_read_b64_tr_b16 v[152:153], %5 offset:46080\n"
        "ds_read_b64_tr_b16 v[154:155], %5 offset:46464\n"
        "s_waitcnt lgkmcnt(8)\n"
        "v_mfma_f32_32x32x16_bf16 %2, v[160:163], v[208:211], %2\n"
        "s_waitcnt vmcnt(7)\n"
        "v_permlane16_swap_b32 v212, v214\n"
        "v_permlane16_swap_b32 v213, v215\n"
        "ds_read_b64_tr_b16 v[156:157], %5 offset:46112\n"
        "ds_read_b64_tr_b16 v[158:159], %5 offset:46496\n"
        "s_waitcnt lgkmcnt(8)\n"
        "v_mfma_f32_32x32x16_bf16 %0, v[164:167], v[212:215], %0\n"
        "ds_read_b64_tr_b16 v[160:161], %5 offset:46144\n"
        "ds_read_b64_tr_b16 v[162:163], %5 offset:46528\n"
        "buffer_load_dwordx4 v[244:247], %4, %7, s40 offen offset:1024 sc1\n"
        "s_waitcnt lgkmcnt(8)\n"
        "v_mfma_f32_32x32x16_bf16 %1, v[168:171], v[212:215], %1\n"
        "ds_read_b64_tr_b16 v[164:165], %5 offset:47616\n"
        "ds_read_b64_tr_b16 v[166:167], %5 offset:48000\n"
        "s_waitcnt lgkmcnt(8)\n"
        "v_mfma_f32_32x32x16_bf16 %2, v[172:175], v[212:215], %2\n"
        "s_waitcnt vmcnt(7)\n"
        "v_permlane16_swap_b32 v216, v218\n"
        "v_permlane16_swap_b32 v217, v219\n"
        "ds_read_b64_tr_b16 v[168:169], %5 offset:47648\n"
        "ds_read_b64_tr_b16 v[170:171], %5 offset:48032\n"
        "s_waitcnt lgkmcnt(8)\n"
        "v_mfma_f32_32x32x16_bf16 %0, v[152:155], v[216:219], %0\n"
        "ds_read_b64_tr_b16 v[172:173], %5 offset:47680\n"
        "ds_read_b64_tr_b16 v[174:175], %5 offset:48064\n"
        "buffer_load_dwordx4 v[248:251], %4, %7, s40 offen offset:2048 sc1\n"
        "s_waitcnt lgkmcnt(8)\n"
        "v_mfma_f32_32x32x16_bf16 %1, v[156:159], v[216:219], %1\n"
        "ds_read_b64_tr_b16 v[152:153], %5 offset:49152\n"
        "ds_read_b64_tr_b16 v[154:155], %5 offset:49536\n"
        "s_waitcnt lgkmcnt(8)\n"
        "v_mfma_f32_32x32x16_bf16 %2, v[160:163], v[216:219], %2\n"
        "s_waitcnt vmcnt(7)\n"
        "v_permlane16_swap_b32 v220, v222\n"
        "v_permlane16_swap_b32 v221, v223\n"
        "ds_read_b64_tr_b16 v[156:157], %5 offset:49184\n"
        "ds_read_b64_tr_b16 v[158:159], %5 offset:49568\n"
        "s_waitcnt lgkmcnt(8)\n"
        "v_mfma_f32_32x32x16_bf16 %0, v[164:167], v[220:223], %0\n"
        "ds_read_b64_tr_b16 v[160:161], %5 offset:49216\n"
        "ds_read_b64_tr_b16 v[162:163], %5 offset:49600\n"
        "buffer_load_dwordx4 v[252:255], %4, %7, s40 offen offset:3072 sc1\n"
        "s_waitcnt lgkmcnt(8)\n"
        "v_mfma_f32_32x32x16_bf16 %1, v[168:171], v[220:223], %1\n"
        "ds_read_b64_tr_b16 v[164:165], %5 offset:50688\n"
        "ds_read_b64_tr_b16 v[166:167], %5 offset:51072\n"
        "s_waitcnt lgkmcnt(8)\n"
        "v_mfma_f32_32x32x16_bf16 %2, v[172:175], v[220:223], %2\n"
        "s_waitcnt vmcnt(7)\n"
        "v_permlane16_swap_b32 v224, v226\n"
        "v_permlane16_swap_b32 v225, v227\n"
        "ds_read_b64_tr_b16 v[168:169], %5 offset:50720\n"
        "ds_read_b64_tr_b16 v[170:171], %5 offset:51104\n"
        "s_waitcnt lgkmcnt(8)\n"
        "v_mfma_f32_32x32x16_bf16 %0, v[152:155], v[224:227], %0\n"
        "ds_read_b64_tr_b16 v[172:173], %5 offset:50752\n"
        "ds_read_b64_tr_b16 v[174:175], %5 offset:51136\n"
        "s_waitcnt lgkmcnt(8)\n"
        "v_mfma_f32_32x32x16_bf16 %1, v[156:159], v[224:227], %1\n"
        "ds_read_b64_tr_b16 v[152:153], %5 offset:52224\n"
        "ds_read_b64_tr_b16 v[154:155], %5 offset:52608\n"
        "s_waitcnt lgkmcnt(8)\n"
        "v_mfma_f32_32x32x16_bf16 %2, v[160:163], v[224:227], %2\n"
        "s_waitcnt vmcnt(6)\n"
        "v_permlane16_swap_b32 v228, v230\n"
        "v_permlane16_swap_b32 v229, v231\n"
        "ds_read_b64_tr_b16 v[156:157], %5 offset:52256\n"
        "ds_read_b64_tr_b16 v[158:159], %5 offset:52640\n"
        "s_waitcnt lgkmcnt(8)\n"
        "v_mfma_f32_32x32x16_bf16 %0, v[164:167], v[228:231], %0\n"
        "ds_read_b64_tr_b16 v[160:161], %5 offset:52288\n"
        "ds_read_b64_tr_b16 v[162:163], %5 offset:52672\n"
        "s_waitcnt lgkmcnt(8)\n"
        "v_mfma_f32_32x32x16_bf16 %1, v[168:171], v[228:231], %1\n"
        "ds_read_b64_tr_b16 v[164:165], %5 offset:53760\n"
        "ds_read_b64_tr_b16 v[166:167], %5 offset:54144\n"
        "s_waitcnt lgkmcnt(8)\n"
        "v_mfma_f32_32x32x16_bf16 %2, v[172:175], v[228:231], %2\n"
        "s_waitcnt vmcnt(5)\n"
        "v_permlane16_swap_b32 v232, v234\n"
        "v_permlane16_swap_b32 v233, v235\n"
        "ds_read_b64_tr_b16 v[168:169], %5 offset:53792\n"
        "ds_read_b64_tr_b16 v[170:171], %5 offset:54176\n"
        "s_waitcnt lgkmcnt(8)\n"
        "v_mfma_f32_32x32x16_bf16 %0, v[152:155], v[232:235], %0\n"
        "ds_read_b64_tr_b16 v[172:173], %5 offset:53824\n"
        "ds_read_b64_tr_b16 v[174:175], %5 offset:54208\n"
        "buffer_load_dwordx2 %3, %6, %7, 0 offen sc1\n"
        "s_waitcnt lgkmcnt(8)\n"
        "v_mfma_f32_32x32x16_bf16 %1, v[156:159], v[232:235], %1\n"
        "ds_read_b64_tr_b16 v[152:153], %5 offset:55296\n"
        "ds_read_b64_tr_b16 v[154:155], %5 offset:55680\n"
        "s_waitcnt lgkmcnt(8)\n"
        "v_mfma_f32_32x32x16_bf16 %2, v[160:163], v[232:235], %2\n"
        "s_waitcnt vmcnt(5)\n"
        "v_permlane16_swap_b32 v236, v238\n"
        "v_permlane16_swap_b32 v237, v239\n"
        "ds_read_b64_tr_b16 v[156:157], %5 offset:55328\n"
        "ds_read_b64_tr_b16 v[158:159], %5 offset:55712\n"
        "s_waitcnt lgkmcnt(8)\n"
        "v_mfma_f32_32x32x16_bf16 %0, v[164:167], v[236:239], %0\n"
        "ds_read_b64_tr_b16 v[160:161], %5 offset:55360\n"
        "ds_read_b64_tr_b16 v[162:163], %5 offset:55744\n"
        "s_waitcnt lgkmcnt(8)\n"
        "v_mfma_f32_32x32x16_bf16 %1, v[168:171], v[236:239], %1\n"
        "ds_read_b64_tr_b16 v[164:165], %5 offset:56832\n"
        "ds_read_b64_tr_b16 v[166:167], %5 offset:57216\n"
        "s_waitcnt lgkmcnt(8)\n"
        "v_mfma_f32_32x32x16_bf16 %2, v[172:175], v[236:239], %2\n"
        "s_waitcnt vmcnt(4)\n"
        "v_permlane16_swap_b32 v240, v242\n"
        "v_permlane16_swap_b32 v241, v243\n"
        "ds_read_b64_tr_b16 v[168:169], %5 offset:56864\n"
        "ds_read_b64_tr_b16 v[170:171], %5 offset:57248\n"
        "s_waitcnt lgkmcnt(8)\n"
        "v_mfma_f32_32x32x16_bf16 %0, v[152:155], v[240:243], %0\n"
        "ds_read_b64_tr_b16 v[172:173], %5 offset:56896\n"
        "ds_read_b64_tr_b16 v[174:175], %5 offset:57280\n"
        "s_waitcnt lgkmcnt(8)\n"
        "v_mfma_f32_32x32x16_bf16 %1, v[156:159], v[240:243], %1\n"
        "ds_read_b64_tr_b16 v[152:153], %5 offset:58368\n"
        "ds_read_b64_tr_b16 v[154:155], %5 offset:58752\n"
        "s_waitcnt lgkmcnt(8)\n"
        "v_mfma_f32_32x32x16_bf16 %2, v[160:163], v[240:243], %2\n"
        "s_waitcnt vmcnt(3)\n"
        "v_permlane16_swap_b32 v244, v246\n"
        "v_permlane16_swap_b32 v245, v247\n"
        "ds_read_b64_tr_b16 v[156:157], %5 offset:58400\n"
        "ds_read_b64_tr_b16 v[158:159], %5 offset:58784\n"
        "s_waitcnt lgkmcnt(8)\n"
        "v_mfma_f32_32x32x16_bf16 %0, v[164:167], v[244:247], %0\n"
        "ds_read_b64_tr_b16 v[160:161], %5 offset:58432\n"
        "ds_read_b64_tr_b16 v[162:163], %5 offset:58816\n"
        "s_waitcnt lgkmcnt(8)\n"
        "v_mfma_f32_32x32x16_bf16 %1, v[168:171], v[244:247], %1\n"
        "ds_read_b64_tr_b16 v[164:165], %5 offset:59904\n"
        "ds_read_b64_tr_b16 v[166:167], %5 offset:60288\n"
        "s_waitcnt lgkmcnt(8)\n"
        "v_mfma_f32_32x32x16_bf16 %2, v[172:175], v[244:247], %2\n"
        "s_waitcnt vmcnt(2)\n"
        "v_permlane16_swap_b32 v248, v250\n"
        "v_permlane16_swap_b32 v249, v251\n"
        "ds_read_b64_tr_b16 v[168:169], %5 offset:59936\n"
        "ds_read_b64_tr_b16 v[170:171], %5 offset:60320\n"
        "s_waitcnt lgkmcnt(8)\n"
        "v_mfma_f32_32x32x16_bf16 %0, v[152:155], v[248:251], %0\n"
        "ds_read_b64_tr_b16 v[172:173], %5 offset:59968\n"
        "ds_read_b64_tr_b16 v[174:175], %5 offset:60352\n"
        "s_waitcnt lgkmcnt(8)\n"
        "v_mfma_f32_32x32x16_bf16 %1, v[156:159], v[248:251], %1\n"
        "s_waitcnt lgkmcnt(6)\n"
        "v_mfma_f32_32x32x16_bf16 %2, v[160:163], v[248:251], %2\n"
        "s_waitcnt vmcnt(1)\n"
        "v_permlane16_swap_b32 v252, v254\n"
        "v_permlane16_swap_b32 v253, v255\n"
        "s_nop 1\n"
        "s_waitcnt lgkmcnt(4)\n"
        "v_mfma_f32_32x32x16_bf16 %0, v[164:167], v[252:255], %0\n"
        "s_waitcnt lgkmcnt(2)\n"
        "v_mfma_f32_32x32x16_bf16 %1, v[168:171], v[252:255], %1\n"
        "s_waitcnt lgkmcnt(0)\n"
        "v_mfma_f32_32x32x16_bf16 %2, v[172:175], v[252:255], %2\n"
        "s_waitcnt vmcnt(0)\n"
        "s_nop 7\n"
        "s_nop 3\n"
        : "=&v"(acc[0]), "=&v"(acc[1]), "=&v"(acc[2]), "=&v"(st2)
        : "v"(hoff), "v"(fb), "v"(soff), "s"(wsr)
        : "memory", "s40", "v152", "v153", "v154", "v155", "v156", "v157", "v158", "v159", "v160", "v161", "v162", "v163", "v164", "v165", "v166", "v167", "v168", "v169", "v170", "v171", "v172", "v173", "v174", "v175", "v176", "v177", "v178", "v179", "v180", "v181", "v182", "v183", "v184", "v185", "v186", "v187", "v188", "v189", "v190", "v191", "v192", "v193", "v194", "v195", "v196", "v197", "v198", "v199", "v200", "v201", "v202", "v203", "v204", "v205", "v206", "v207", "v208", "v209", "v210", "v211", "v212", "v213", "v214", "v215", "v216", "v217", "v218", "v219", "v220", "v221", "v222", "v223", "v224", "v225", "v226", "v227", "v228", "v229", "v230", "v231", "v232", "v233", "v234", "v235", "v236", "v237", "v238", "v239", "v240", "v241", "v242", "v243", "v244", "v245", "v246", "v247", "v248", "v249", "v250", "v251", "v252", "v253", "v254", "v255");

                }
            }
            STAMP(9);
            const int h = lane >> 5;
            u4 sv2[5];
            if (LN2E && !TH && w == 3) { await(smem, fB, 1); ln2_load(sv2, wsr, stok + 1024, lane); }
            await(smem, cCS, 1);
            if (!TH) await(smem, cPk, 1);
            f4 cs[3][4];
#pragma unroll
            for (int T = 0; T < 3; ++T) {
                const float* cg = (const float*)(smem + G1_CS) + 16 * T + 8 * h;
                cs[T][0] = *(const f4*)cg; cs[T][1] = *(const f4*)(cg + 4); cs[T][2] = *(const f4*)(cg + 48); cs[T][3] = *(const f4*)(cg + 52);
            }
            sig(smem, cL, lane);
            __builtin_amdgcn_s_setprio(TH ? 2 : EPRIO);
            POLL {
                if (__all(st2[1] == tagv)) break;
                __builtin_amdgcn_s_sleep(1);
                FENCE();
                st2 = ld8(wsr, WS_STAT2 + tok * 8);
            }
            const float nmean = -bf_lo(st2[0]), sd = bf_hi(st2[0]);
            const float ce = -1.44269504088896341f * __builtin_amdgcn_rcpf(sd), cv = sd * sd;
            const int ooff = WS_HB + (tok >> 4) * HBS + (ub0 * 2 + h) * 256 + (tok & 15) * 16;
#pragma unroll
            for (int T = 0; T < 3; ++T) {
                u4 o;
                if (TH) {
#pragma unroll
                    for (int j = 0; j < 4; ++j) {
                        const f2 cgj = (j & 1) ? cs[T][j >> 1].zw : cs[T][j >> 1].xy;
                        const f2 cuj = (j & 1) ? cs[T][2 + (j >> 1)].zw : cs[T][2 + (j >> 1)].xy;
                        const f2 G = f2{acc[T][2 * j], acc[T][2 * j + 1]} + cgj * nmean;
                        const f2 U = f2{acc[T][8 + 2 * j], acc[T][9 + 2 * j]} + cuj * nmean;
                        const f2 e = G * ce;
                        const f2 den = f2{__builtin_amdgcn_exp2f(e.x), __builtin_amdgcn_exp2f(e.y)} * cv + cv;
                        o[j] = pk_bf(G * U * f2{__builtin_amdgcn_rcpf(den.x), __builtin_amdgcn_rcpf(den.y)});
                    }
                } else {
                    if (LN2E && T == 1 && w == 3) ln2_put(wsr, stok + 1024, ln2_calc(sv2, lane), tagv, lane);
                    float G[8], U[8], e[8];
#define EACH8 _Pragma("unroll") for (int j = 0; j < 8; ++j)
#define WLD(k) do { SB(); _Pragma("unroll") for (int q_ = 0; q_ < WPS; ++q_) { const int f = NE0 + (T * 6 + k) * WPS + q_; if (f < 35) wf[f] = ld16(wsr, woff + (f & 3) * 1024, (f >> 2) * 4096); } SB(); } while (0)
                    WLD(0);
                    EACH8 G[j] = fmaS(cs[T][j >> 2][j & 3], nmean, acc[T][j]);
                    WLD(1);
                    EACH8 e[j] = mulS(G[j], ce);
                    WLD(2);
                    EACH8 { e[j] = __builtin_amdgcn_exp2f(e[j]); U[j] = fmaS(cs[T][2 + (j >> 2)][j & 3], nmean, acc[T][8 + j]); }
                    WLD(3);
                    EACH8 e[j] = fmaS(e[j], cv, cv);
                    WLD(4);
                    EACH8 { e[j] = __builtin_amdgcn_rcpf(e[j]); G[j] = mulS(G[j], U[j]); }
                    WLD(5);
                    EACH8 G[j] = mulS(G[j], e[j]);
                    SB();
                    o = u4{pk_bf(G[0], G[1]), pk_bf(G[2], G[3]), pk_bf(G[4], G[5]), pk_bf(G[6], G[7])};
                }
                st16(wsr, ooff + T * ubs * 512, o);
            }
            if (TH) sig(smem, cP + kq, lane);
            STAMP(10);
            if (TH) {
#pragma unroll
                for (int f = LP0; f < LP0 + LPRE; ++f) { wf[f] = ld16(wsr, woff + (f & 3) * 1024, (f >> 2) * 4096); SB(); }
                asm volatile("s_waitcnt vmcnt(%0)" :: "i"(LPRE) : "memory");
            } else WAITV(0);
            if (sig_ret(smem, cD2A + TH, lane) == 3u && lane == 0) st4(wsr, WS_FLAG2 + TH * 16384 + (mb * 32 + n1) * 64, tagv);
            __builtin_amdgcn_s_setprio(0);
            STAMP(11);
            lane = lane_id();
            auto ldres = [&](u2 (&hres)[3], int tb2, int tl0, int tls) {
#pragma unroll
                for (int i = 0; i < (TH ? 1 : 2); ++i) {
                    const int tl = tl0 + i * tls;
                    hres[i] = ld8(wsr, tl < 10 ? WS_H4 + tb2 * HS + (nt * 20 + 4 * (tl >> 1) + (lane >> 4)) * 256 + (16 * (tl & 1) + (lane & 15)) * 8 : WS_END);
                }
            };
            const int tlb = w ^ 1;
            u2 hres0[3], hres1[3];
            ldres(hres0, mt, tlb, 8);
            ldres(hres1, 32 + mt, tlb, 8);
            SB();
            {
                const int woff = WS_P2 + nt * P2S + s0 * 5120 + lane * 16;
#pragma unroll
                for (int f = TH ? LP0 + LPRE : NE0 + 18 * WPS; f < NSW * 5; ++f) { wf[f] = ld16(wsr, woff + (f & 3) * 1024, (f >> 2) * 4096); if (!TH) PACE(); SB(); }
                TST(7);
            }
            f4 ac2[5][2];
            auto finish = [&](int ph, int tl0, int tls, const u2 (&hres)[3]) {
#pragma unroll
                for (int i = 0; i < (TH ? 1 : 2); ++i) {
                    const int tl = tl0 + i * tls;
                    if (tl < 10) {
                        const f4* p = (const f4*)smem + ph * 5120 + tl * 64 + lane;
                        f4 q[8], o;
#pragma unroll
                        for (int k = 0; k < 8; ++k) q[k] = p[640 * k];
                        if (!TH && ph && tl == 9) q[7] = ac2[4][1];
                        SB();
#pragma unroll
                        for (int j = 0; j < 4; ++j)
                            o[j] = addS(addS(addS(addS(q[0][j], q[1][j]), addS(q[2][j], q[3][j])), addS(addS(q[4][j], q[5][j]), addS(q[6][j], q[7][j]))),
                                        (j & 1) ? bf_hi(hres[i][j >> 1]) : bf_lo(hres[i][j >> 1]));
                        const int tok2 = (ph * 32 + mt) * 32 + 16 * (tl & 1) + (lane & 15);
                        *(u2*)(out + (size_t)tok2 * D + nt * 80 + 16 * (tl >> 1) + 4 * (lane >> 4)) = u2{pk_bf(o[0], o[1]), pk_bf(o[2], o[3])};
                    }
                }
            };
            u4 tk[NSW][2];
            auto tokens = [&](int ph) {
                if (TH) awaitf(smem, cS + ph * 8 + kq, 1);
                else {
                    POLL {
                        FENCE();
                        if (__all(ld4(wsr, WS_FLAG2 + ph * 16384 + ((mt >> 2) * 32 + kq * 8 + (lane & 7)) * 64) == tagv)) break;
                        __builtin_amdgcn_s_sleep(2);
                    }
                    sig(smem, cS + ph * 8 + kq, lane);
                }
                const int hoff2 = WS_HB + 2 * (ph * 32 + mt) * HBS + s0 * 1024 + lane * 16;
#pragma unroll
                for (int s = 0; s < NSW; ++s)
#pragma unroll
                    for (int e = 0; e < 2; ++e) { tk[s][e] = ld16(wsr, hoff2 + s * 1024, e * HBS); SB(); }
            };
            auto mm = [&]() {
#pragma unroll
                for (int s = 0; s < NSW; ++s) {
#pragma unroll
                    for (int T = 0; T < 5; ++T)
#pragma unroll
                        for (int e = 0; e < 2; ++e)
                            ac2[T][e] = __builtin_amdgcn_mfma_f32_16x16x32_bf16(__builtin_bit_cast(b8, wf[s * 5 + T]), __builtin_bit_cast(b8, tk[s][e]), s ? ac2[T][e] : f4{0.f, 0.f, 0.f, 0.f}, 0, 0, 0);
                    SB();
                }
            };
            auto park = [&](int ph) {
                f4* red = (f4*)smem + (ph * 8 + 7 - w) * 640 + lane;
#pragma unroll
                for (int tl = 0; tl < 10; ++tl) if (!(!TH && ph && tl == 9) || w != 0) red[tl * 64] = ac2[tl >> 1][tl & 1];
                sig(smem, cRedA + ph, lane);
            };

            tokens(0);
            SB();
            if (!TH) await(smem, cP + kq, 1);
            TST(1);
            mm();
            TST(2);
            lane = lane_id();
            awaitf(smem, cL, 8);
            park(0);
            TST(3);
            tokens(1);
            const bool early_fin = peek(smem, cRedA) >= 8u;
            if (early_fin) finish(0, tlb, 8, hres0);
            mm();
            TST(5);
            lane = lane_id();
            park(1);
            if (!early_fin) { awaitf(smem, cRedA, 8); finish(0, tlb, 8, hres0); }
            TST(4);
            awaitf(smem, cRedB, 8);
            TST(6);
            finish(1, tlb, 8, hres1);
        };
        if (th) rest(std::integral_constant<int, 1>{}); else rest(std::integral_constant<int, 0>{});
        STAMP(14);
        asm volatile(".Lk_all_end:" ::: "memory");
    }
}

extern "C" void kernel_launch(void* const* d_in, const int* in_sizes, int n_in, void* d_out, int out_size, void* d_ws, size_t ws_size, hipStream_t stream) {
    (void)hipFuncSetAttribute((const void*)k_all, hipFuncAttributeMaxDynamicSharedMemorySize, LDS_BYTES);
    k_all<<<256, 512, LDS_BYTES, stream>>>((const u16*)d_in[0], (const u16*)d_in[1], (const u16*)d_in[2], (const u16*)d_in[3], (const u16*)d_in[4],
                                           (const u16*)d_in[5], (const u16*)d_in[6], (const u16*)d_in[8], (const u16*)d_in[9], (const u16*)d_in[10],
                                           (char*)d_ws, (u16*)d_out);
}
```
